```python
import torch

def _softmax(x, axis=-1):
    return torch.softmax(x, dim=axis)

def _gelu(x):
    return torch.nn.functional.gelu(x, approximate='tanh')

def _silu(x):
    return torch.nn.functional.silu(x)

def _sigmoid(x):
    return torch.sigmoid(x)

def _layernorm(x, axis=-1, eps=1e-05):
    m = x.mean(dim=axis, keepdim=True)
    v = x.var(dim=axis, keepdim=True, unbiased=False)
    return (x - m) / torch.sqrt(v + eps)

def _scatter_add(tgt, idx, src, axis=0):
    return tgt.clone().index_add_(axis, idx, src)

def _segment_sum(src, seg, n):
    out = torch.zeros((n,) + src.shape[1:], dtype=src.dtype)
    return out.index_add_(0, seg, src)

def _pad(x, pw):
    flat = tuple((v for pair in reversed(pw) for v in pair))
    return torch.nn.functional.pad(x, flat)
kernel_name = 'adaLN_cond__ssm_conv_h3__mixer_gmlp__prompt'

def setup_inputs():

    _w = lambda a: (lambda t: t.to(torch.bfloat16) if t.is_floating_point() else t)(torch.as_tensor(a))

    def _A_setup():
        B, S, D = (1, 2048, 448)
        ks = 4
        return {'q': _w((lambda *s: torch.randn(*s, dtype=torch.bfloat16) * 0.05)(B, S, D)), 'k': _w((lambda *s: torch.randn(*s, dtype=torch.bfloat16) * 1.6)(B, S, D)), 'v': _w((lambda *s: torch.randn(*s, dtype=torch.bfloat16) * 1.6)(B, S, D)), 'shift_k': _w((lambda *s: torch.randn(*s, dtype=torch.bfloat16) * 1.6)(D, ks) * 0.5), 'ssm_k': _w((lambda *s: torch.randn(*s, dtype=torch.bfloat16) * 1.6)(D, S) * (1.0 / S ** 0.5)), 'ssm_D': _w((lambda *s: torch.randn(*s, dtype=torch.bfloat16) * 1.6)(D))}
    _A_in = _A_setup()

    def _B_setup():
        B, S, D, F = (1, 2048, 448, 1792)
        Fh = F // 2
        return {'x': _w((lambda *s: torch.randn(*s, dtype=torch.bfloat16) * 0.05)(B, S, D)), 'w_in': _w((lambda *s: torch.randn(*s, dtype=torch.bfloat16) * 0.8)(D, 2 * Fh)), 'w_sp': _w(0.01 * (lambda *s: torch.randn(*s, dtype=torch.bfloat16) * 0.8)(S, S)), 'b_sp': _w(1.0 + 0.01 * (lambda *s: torch.randn(*s, dtype=torch.bfloat16) * 0.1)(S)), 'w_out': _w((lambda *s: torch.randn(*s, dtype=torch.bfloat16) * 0.1)(Fh, D))}
    _B_in = _B_setup()
    return {'A_q': _A_in['q'], 'A_k': _A_in['k'], 'A_v': _A_in['v'], 'A_shift_k': _A_in['shift_k'], 'A_ssm_k': _A_in['ssm_k'], 'A_ssm_D': _A_in['ssm_D'], 'B_x': _B_in['x'], 'B_w_in': _B_in['w_in'], 'B_w_sp': _B_in['w_sp'], 'B_b_sp': _B_in['b_sp'], 'B_w_out': _B_in['w_out'], 'c': _w((lambda *s: torch.randn(*s, dtype=torch.bfloat16) * 0.1)(1, 448) * 8.0), 'w_mod': _w((lambda *s: torch.randn(*s, dtype=torch.bfloat16) * 0.1)(448, 6 * 448) * 448 ** (-0.5) * 8.0)}

def reference(A_q, A_k, A_v, A_shift_k, A_ssm_k, A_ssm_D, B_x, B_w_in, B_w_sp, B_b_sp, B_w_out, c, w_mod):

    def _A_ref(q, k, v, shift_k, ssm_k, ssm_D):
        B, S, D = q.shape
        ks = shift_k.shape[-1]
        k_pad = _pad(k, ((0, 0), (ks - 1, 0), (0, 0)))
        k_shift = torch.zeros_like(k)
        for t in range(ks):
            k_shift = k_shift + k_pad[:, t:t + S, :] * shift_k[:, ks - 1 - t][None, None, :]
        kv = k_shift * v
        L = 2 * S
        kv_pad = _pad(kv, ((0, 0), (0, S), (0, 0)))
        kf_pad = _pad(ssm_k, ((0, 0), (0, S)))
        kv_f = (lambda x, axes=(-2, -1): torch.fft.rfft2(x.float(), dim=axes))(kv_pad[:, None, :, :], axes=(1, 2))
        kf_f = (lambda x, axes=(-2, -1): torch.fft.rfft2(x.float(), dim=axes))(kf_pad[:, None, :], axes=(1, 2))
        kf_f = (lambda x, *d: x.permute(*d))(kf_f, 1, 2, 0)
        y_f = kv_f * kf_f[None, :, :, :]
        y = (lambda x, s, axes=(-2, -1): torch.fft.irfft2(x, s=s, dim=axes).to(torch.bfloat16))(y_f, (1, L), axes=(1, 2))[:, 0, :S, :]
        y = y + kv * ssm_D[None, None, :]
        return q * y

    def _B_ref(x, w_in, w_sp, b_sp, w_out):
        B, S, D = x.shape
        F = w_in.shape[-1]
        Fh = F // 2
        z = _gelu(torch.einsum('bsd,df->bsf', x, w_in))
        z1 = z[..., :Fh].clone()
        z2 = _layernorm(z[..., Fh:], axis=-1)
        g = torch.einsum('ts,bsf->btf', w_sp, z2) + b_sp[None, :, None]
        return torch.einsum('bsf,fd->bsd', z1 * g, w_out)
    x = A_q
    mod = (_silu(c) @ w_mod).reshape(c.shape[0], 6, 1, -1)
    sa, ca, ga, sb, cb, gb = (mod[:, 0].clone(), mod[:, 1].clone(), mod[:, 2].clone(), mod[:, 3].clone(), mod[:, 4].clone(), mod[:, 5].clone())
    h = _layernorm(x, axis=-1) * (1 + ca) + sa
    x = x + ga * _A_ref(h, A_k, A_v, A_shift_k, A_ssm_k, A_ssm_D)
    h = _layernorm(x, axis=-1) * (1 + cb) + sb
    x = x + gb * _B_ref(h, B_w_in, B_w_sp, B_b_sp, B_w_out)
    return x
```

```cpp
#include <hip/hip_runtime.h>
#include <hip/hip_bf16.h>
#include <stdint.h>

#define S_LEN 2048
#define D_DIM 448
#define F_DIM 1792
#define FH_DIM 896
#define MOD_N 2688

typedef unsigned short u16;
typedef __attribute__((ext_vector_type(8))) short s16x8;
typedef __attribute__((ext_vector_type(4))) short s16x4;
typedef __attribute__((ext_vector_type(4))) float f32x4;
typedef __attribute__((ext_vector_type(16))) float f32x16;
typedef __attribute__((ext_vector_type(4))) unsigned u32x4;
typedef __attribute__((ext_vector_type(2))) unsigned u32x2;

__device__ __forceinline__ float bf2f(u16 x) { return __uint_as_float(((unsigned)x) << 16); }
__device__ __forceinline__ u16 f2bf(float f) {
  __hip_bfloat16 b = __float2bfloat16(f);
  return *reinterpret_cast<u16*>(&b);
}
__device__ __forceinline__ float rbf(float f) { return bf2f(f2bf(f)); }
__device__ __forceinline__ float lo16(unsigned u) { return __uint_as_float(u << 16); }
__device__ __forceinline__ float hi16(unsigned u) { return __uint_as_float(u & 0xffff0000u); }
__device__ __forceinline__ unsigned pack2(float a, float b) {
  return (unsigned)f2bf(a) | ((unsigned)f2bf(b) << 16);
}

#define K1_CONV_BLOCKS 224
#define K1_MOD_BLOCKS 21
#define U_BLK_STRIDE 80
#define U_BYTES (96 * U_BLK_STRIDE)
#define KR_STRIDE 4160
#define CH_BYTES 40960
#define KR_OFF 7680
#define RED_OFF 8192

__global__ __launch_bounds__(512, 1) void k1_conv_mod(
    const u16* __restrict__ kin, const u16* __restrict__ vin, const u16* __restrict__ shiftk,
    const u16* __restrict__ ssmk, const u16* __restrict__ ssmD, const u16* __restrict__ cvec,
    const u16* __restrict__ wmod, float* __restrict__ mod_out, u16* __restrict__ yT) {
  extern __shared__ __attribute__((aligned(16))) char smem[];
  const int tid = threadIdx.x;
  const int lane = tid & 63;
  const int wid = tid >> 6;

  if (blockIdx.x >= K1_CONV_BLOCKS) {
    const int cb = blockIdx.x - K1_CONV_BLOCKS;
    float* red = (float*)smem;
    float* sc = (float*)(smem + 8 * 128 * 4);
    if (tid < D_DIM) {
      float x = bf2f(cvec[tid]);
      sc[tid] = rbf(x / (1.0f + __expf(-x)));
    }
    __syncthreads();
    const int col = cb * 128 + lane * 2;
    float a0 = 0.f, a1 = 0.f;
    const int r0 = wid * 56;
#pragma unroll 8
    for (int r = 0; r < 56; ++r) {
      unsigned w = *(const unsigned*)(wmod + (size_t)(r0 + r) * MOD_N + col);
      float s = sc[r0 + r];
      a0 += s * lo16(w);
      a1 += s * hi16(w);
    }
    red[wid * 128 + lane * 2] = a0;
    red[wid * 128 + lane * 2 + 1] = a1;
    __syncthreads();
    if (tid < 128) {
      float s = 0.f;
#pragma unroll
      for (int w = 0; w < 8; ++w) s += red[w * 128 + tid];
      mod_out[cb * 128 + tid] = rbf(s);
    }
    return;
  }

  const int d0 = blockIdx.x * 2;
  for (int i = tid; i < 2 * 32 * U_BLK_STRIDE / 4; i += 512) {
    int ch = i / (32 * U_BLK_STRIDE / 4);
    int o = i % (32 * U_BLK_STRIDE / 4);
    *(unsigned*)(smem + ch * CH_BYTES + o * 4) = 0u;
  }
  {
    const int ch = tid >> 8;
    const int t = tid & 255;
    u32x4 kk = *(const u32x4*)(ssmk + (size_t)(d0 + ch) * S_LEN + t * 8);
    char* kr = smem + ch * CH_BYTES + KR_OFF;
    u16 taps[8];
#pragma unroll
    for (int e = 0; e < 4; ++e) { taps[2 * e] = (u16)(kk[e] & 0xffff); taps[2 * e + 1] = (u16)(kk[e] >> 16); }
#pragma unroll
    for (int e = 0; e < 8; ++e) {
      int p = 2047 - (t * 8 + e);
#pragma unroll
      for (int s = 0; s < 4; ++s) {
        int q = p - s;
        if (q >= 0) *(u16*)(kr + s * KR_STRIDE + q * 2) = taps[e];
      }
    }
    if (tid < 2 * 4 * 32) {
      int c2 = tid >> 7, s = (tid >> 5) & 3, e = tid & 31;
      int q = 2048 + e - s;
      *(u16*)(smem + c2 * CH_BYTES + KR_OFF + s * KR_STRIDE + q * 2) = 0;
    }
  }
  {
    const int s0 = tid * 4;
    unsigned kr[4], vr[4], kh[3];
#pragma unroll
    for (int i = 0; i < 4; ++i) {
      kr[i] = *(const unsigned*)(kin + (size_t)(s0 + i) * D_DIM + d0);
      vr[i] = *(const unsigned*)(vin + (size_t)(s0 + i) * D_DIM + d0);
    }
#pragma unroll
    for (int i = 0; i < 3; ++i) {
      unsigned fromprev = __shfl_up(kr[i + 1], 1);
      unsigned g = 0u;
      if (lane == 0) {
        int s = s0 - 3 + i;
        if (s >= 0) g = *(const unsigned*)(kin + (size_t)s * D_DIM + d0);
      }
      kh[i] = (lane == 0) ? g : fromprev;
    }
    float sk0[4], sk1[4];
#pragma unroll
    for (int j = 0; j < 4; ++j) {
      sk0[j] = bf2f(shiftk[(d0)*4 + j]);
      sk1[j] = bf2f(shiftk[(d0 + 1) * 4 + j]);
    }
    unsigned kall[7] = {kh[0], kh[1], kh[2], kr[0], kr[1], kr[2], kr[3]};
#pragma unroll
    for (int i = 0; i < 4; ++i) {
      float a0 = 0.f, a1 = 0.f;
#pragma unroll
      for (int t = 0; t < 4; ++t) {
        a0 = rbf(a0 + rbf(lo16(kall[i + t]) * sk0[3 - t]));
        a1 = rbf(a1 + rbf(hi16(kall[i + t]) * sk1[3 - t]));
      }
      float kv0 = rbf(a0 * lo16(vr[i]));
      float kv1 = rbf(a1 * hi16(vr[i]));
      int s = s0 + i;
      int off = (32 + (s >> 5)) * U_BLK_STRIDE + (s & 31) * 2;
      *(u16*)(smem + off) = f2bf(kv0);
      *(u16*)(smem + CH_BYTES + off) = f2bf(kv1);
    }
  }
  __syncthreads();

  const int ch = wid >> 2;
  const int wq = wid & 3;
  const char* U = smem + ch * CH_BYTES;
  const char* KR = smem + ch * CH_BYTES + KR_OFF;
  const int j = lane & 31;
  const int kg = lane >> 5;
  f32x16 acc0, acc1;
#pragma unroll
  for (int i = 0; i < 16; ++i) { acc0[i] = 0.f; acc1[i] = 0.f; }
  for (int dl = wq; dl < 64; dl += 4) {
#pragma unroll
    for (int h = 0; h < 2; ++h) {
      int n = 32 * dl + j - 16 * h - 8 * kg;
      int p0 = 2047 - n;
      int s = p0 & 3;
      int q = p0 - s;
      const char* ap = KR + s * KR_STRIDE + q * 2;
      u32x2 a_lo = *(const u32x2*)(ap);
      u32x2 a_hi = *(const u32x2*)(ap + 8);
      u32x4 au = {a_lo[0], a_lo[1], a_hi[0], a_hi[1]};
      s16x8 afrag = __builtin_bit_cast(s16x8, au);
      {
        int blk = 32 + (32 + j - dl);
        u32x4 bu = *(const u32x4*)(U + blk * U_BLK_STRIDE + (16 * h + 8 * kg) * 2);
        acc1 = __builtin_amdgcn_mfma_f32_32x32x16_bf16(afrag, __builtin_bit_cast(s16x8, bu), acc1, 0, 0, 0);
      }
      if (dl < 32) {
        int blk = 32 + (j - dl);
        u32x4 bu = *(const u32x4*)(U + blk * U_BLK_STRIDE + (16 * h + 8 * kg) * 2);
        acc0 = __builtin_amdgcn_mfma_f32_32x32x16_bf16(afrag, __builtin_bit_cast(s16x8, bu), acc0, 0, 0, 0);
      }
    }
  }
  __syncthreads();
  {
    float* red = (float*)(smem + ch * CH_BYTES + RED_OFF);
#pragma unroll
    for (int r = 0; r < 16; ++r) {
      int row = (r & 3) + 8 * (r >> 2) + 4 * kg;
      red[((wq * 2 + 0) * 32 + row) * 32 + j] = acc0[r];
      red[((wq * 2 + 1) * 32 + row) * 32 + j] = acc1[r];
    }
  }
  __syncthreads();
  {
    const int c2 = tid >> 8;
    const int t = tid & 255;
    const int sbase = t * 8;
    const float* red = (const float*)(smem + c2 * CH_BYTES + RED_OFF);
    const char* Uc = smem + c2 * CH_BYTES;
    const float Dv = bf2f(ssmD[d0 + c2]);
    u16 outv[8];
#pragma unroll
    for (int e = 0; e < 8; ++e) {
      int s = sbase + e;
      int i = s >> 5, jj = s & 31;
      int tile = i >> 5, col = i & 31;
      float sum = 0.f;
#pragma unroll
      for (int w = 0; w < 4; ++w) sum += red[((w * 2 + tile) * 32 + jj) * 32 + col];
      float y = rbf(sum);
      float kv = bf2f(*(const u16*)(Uc + (32 + i) * U_BLK_STRIDE + jj * 2));
      y = rbf(y + rbf(kv * Dv));
      outv[e] = f2bf(y);
    }
    u32x4 o;
#pragma unroll
    for (int e = 0; e < 4; ++e) o[e] = (unsigned)outv[2 * e] | ((unsigned)outv[2 * e + 1] << 16);
    *(u32x4*)(yT + (size_t)(d0 + c2) * S_LEN + sbase) = o;
  }
}

__device__ __forceinline__ float wave_sum(float v) {
#pragma unroll
  for (int o = 32; o > 0; o >>= 1) v += __shfl_xor(v, o);
  return v;
}

__global__ __launch_bounds__(512) void k2_rowwise(const u16* __restrict__ q, const u16* __restrict__ yT,
                                                  const float* __restrict__ mod, u16* __restrict__ x1out,
                                                  u16* __restrict__ h2out) {
  __shared__ __attribute__((aligned(16))) u16 ys[8][D_DIM + 8];
  const int tid = threadIdx.x;
  const int lane = tid & 63;
  const int wid = tid >> 6;
  const int s0 = blockIdx.x * 8;
  if (tid < D_DIM) {
    u32x4 v = *(const u32x4*)(yT + (size_t)tid * S_LEN + s0);
#pragma unroll
    for (int e = 0; e < 4; ++e) {
      ys[2 * e][tid] = (u16)(v[e] & 0xffff);
      ys[2 * e + 1][tid] = (u16)(v[e] >> 16);
    }
  }
  __syncthreads();
  const int s = s0 + wid;
  const bool act = lane < 56;
  const int c0 = lane * 8;
  float x[8], yv[8];
  float sum = 0.f;
  if (act) {
    u32x4 qv = *(const u32x4*)(q + (size_t)s * D_DIM + c0);
    u32x4 yy = *(const u32x4*)(&ys[wid][c0]);
#pragma unroll
    for (int e = 0; e < 4; ++e) {
      x[2 * e] = lo16(qv[e]); x[2 * e + 1] = hi16(qv[e]);
      yv[2 * e] = lo16(yy[e]); yv[2 * e + 1] = hi16(yy[e]);
    }
#pragma unroll
    for (int e = 0; e < 8; ++e) sum += x[e];
  } else {
#pragma unroll
    for (int e = 0; e < 8; ++e) { x[e] = 0.f; yv[e] = 0.f; }
  }
  float mean = wave_sum(sum) * (1.0f / D_DIM);
  float sq = 0.f;
  if (act) {
#pragma unroll
    for (int e = 0; e < 8; ++e) { float dd = x[e] - mean; sq += dd * dd; }
  }
  float var = wave_sum(sq) * (1.0f / D_DIM);
  float m_b = rbf(mean);
  float sd = rbf(sqrtf(rbf(rbf(var) + 1e-5f)));
  float x1[8];
  float sum2 = 0.f;
  if (act) {
#pragma unroll
    for (int e = 0; e < 8; ++e) {
      int c = c0 + e;
      float sa = mod[0 * D_DIM + c], ca = mod[1 * D_DIM + c], ga = mod[2 * D_DIM + c];
      float n = rbf(rbf(x[e] - m_b) / sd);
      float h = rbf(rbf(n * rbf(1.0f + ca)) + sa);
      float a = rbf(h * yv[e]);
      x1[e] = rbf(x[e] + rbf(ga * a));
      sum2 += x1[e];
    }
  } else {
#pragma unroll
    for (int e = 0; e < 8; ++e) x1[e] = 0.f;
  }
  float mean2 = wave_sum(sum2) * (1.0f / D_DIM);
  float sq2 = 0.f;
  if (act) {
#pragma unroll
    for (int e = 0; e < 8; ++e) { float dd = x1[e] - mean2; sq2 += dd * dd; }
  }
  float var2 = wave_sum(sq2) * (1.0f / D_DIM);
  float m2_b = rbf(mean2);
  float sd2 = rbf(sqrtf(rbf(rbf(var2) + 1e-5f)));
  if (act) {
    u32x4 xo, ho;
    float hh[8];
#pragma unroll
    for (int e = 0; e < 8; ++e) {
      int c = c0 + e;
      float sb = mod[3 * D_DIM + c], cb = mod[4 * D_DIM + c];
      float n = rbf(rbf(x1[e] - m2_b) / sd2);
      hh[e] = rbf(rbf(n * rbf(1.0f + cb)) + sb);
    }
#pragma unroll
    for (int e = 0; e < 4; ++e) {
      xo[e] = pack2(x1[2 * e], x1[2 * e + 1]);
      ho[e] = pack2(hh[2 * e], hh[2 * e + 1]);
    }
    *(u32x4*)(x1out + (size_t)s * D_DIM + c0) = xo;
    *(u32x4*)(h2out + (size_t)s * D_DIM + c0) = ho;
  }
}

struct GemmArgs {
  const u16* A;
  const u16* B;
  int lda, ldb;
  int K, pad;
  u16* out;
  const u16* z;
  float2* stats;
  const u16* bsp;
  const u16* x1;
  const float* mod;
};

__device__ __forceinline__ unsigned a_off(int row, int ch) { return (unsigned)(row * 128 + 16 * (ch ^ (row & 7))); }
template <int BN>
__device__ __forceinline__ unsigned b_off(int row, int ch) {
  if (BN == 128) return (unsigned)(256 * row + 16 * (ch ^ (((row & 3) << 2) | ((row >> 2) & 3))));
  else return (unsigned)(128 * row + 16 * (ch ^ ((((row >> 1) & 1) << 1) | (((row >> 3) & 1) << 2))));
}

__device__ __forceinline__ float gelu_tanh(float x) {
  const float k0 = 0.7978845608028654f, k1 = 0.044715f;
  float u = k0 * (x + k1 * x * x * x);
  float t = 1.0f - 2.0f / (1.0f + __expf(2.0f * u));
  return 0.5f * x * (1.0f + t);
}

template <int BM, int BN, int MODE>
__global__ __launch_bounds__(256) void gemm_kernel(GemmArgs g) {
  constexpr int BK = 64;
  constexpr int WM = BM / 2, WN = BN / 2;
  constexpr int MI = WM / 16, NI = WN / 16;
  constexpr int A_BYTES = BM * BK * 2;
  constexpr int B_BYTES = BK * BN * 2;
  constexpr int A_CH = BM * 8 / 256;
  constexpr int B_CH = BK * (BN / 8) / 256;
  constexpr int CPR = BN / 8;
  extern __shared__ __attribute__((aligned(16))) char smem[];
  char* As = smem;
  char* Bs = smem + 2 * A_BYTES;

  const int tid = threadIdx.x;
  const int lane = tid & 63;
  const int wid = tid >> 6;
  const int wm0 = (wid >> 1) * WM;
  const int wn0 = (wid & 1) * WN;
  const int nbn = gridDim.x;
  (void)nbn;
  const int m0 = blockIdx.y * BM;
  const int n0 = blockIdx.x * BN;
  const int nk = g.K / BK;

  f32x4 acc[MI][NI];
#pragma unroll
  for (int i = 0; i < MI; ++i)
#pragma unroll
    for (int jn = 0; jn < NI; ++jn) acc[i][jn] = (f32x4){0.f, 0.f, 0.f, 0.f};

  u32x4 ar[A_CH], br[B_CH];

  auto load_tiles = [&](int kt) {
    const int k0 = kt * BK;
#pragma unroll
    for (int i = 0; i < A_CH; ++i) {
      int c = tid + 256 * i;
      int row = c >> 3, ch = c & 7;
      ar[i] = *(const u32x4*)(g.A + (size_t)(m0 + row) * g.lda + k0 + ch * 8);
    }
#pragma unroll
    for (int i = 0; i < B_CH; ++i) {
      int c = tid + 256 * i;
      int row = c / CPR, ch = c % CPR;
      br[i] = *(const u32x4*)(g.B + (size_t)(k0 + row) * g.ldb + n0 + ch * 8);
    }
  };
  auto store_tiles = [&](int kt, int buf) {
    const int k0 = kt * BK;
#pragma unroll
    for (int i = 0; i < A_CH; ++i) {
      int c = tid + 256 * i;
      int row = c >> 3, ch = c & 7;
      *(u32x4*)(As + buf * A_BYTES + a_off(row, ch)) = ar[i];
    }
#pragma unroll
    for (int i = 0; i < B_CH; ++i) {
      int c = tid + 256 * i;
      int row = c / CPR, ch = c % CPR;
      u32x4 v = br[i];
      if (MODE == 2) {
        const float2* st = g.stats + (size_t)(k0 + row) * 7;
        float mean = 0.f;
#pragma unroll
        for (int b = 0; b < 7; ++b) mean += st[b].x;
        mean *= (1.0f / 7.0f);
        float m2 = 0.f;
#pragma unroll
        for (int b = 0; b < 7; ++b) { float dd = st[b].x - mean; m2 += st[b].y + 128.0f * dd * dd; }
        float var = m2 * (1.0f / FH_DIM);
        float mb = rbf(mean);
        float sd = rbf(sqrtf(rbf(rbf(var) + 1e-5f)));
#pragma unroll
        for (int e = 0; e < 4; ++e) {
          float a = rbf(rbf(lo16(v[e]) - mb) / sd);
          float b2 = rbf(rbf(hi16(v[e]) - mb) / sd);
          v[e] = pack2(a, b2);
        }
      }
      *(u32x4*)(Bs + buf * B_BYTES + b_off<BN>(row, ch)) = v;
    }
  };

  load_tiles(0);
  store_tiles(0, 0);
  __syncthreads();
  for (int kt = 0; kt < nk; ++kt) {
    const int buf = kt & 1;
    if (kt + 1 < nk) load_tiles(kt + 1);
    const char* Ab = As + buf * A_BYTES;
    const char* Bb = Bs + buf * B_BYTES;
#pragma unroll
    for (int ks = 0; ks < 2; ++ks) {
      s16x8 af[MI], bfr[NI];
#pragma unroll
      for (int i = 0; i < MI; ++i) {
        int row = wm0 + i * 16 + (lane & 15);
        af[i] = *(const s16x8*)(Ab + a_off(row, ks * 4 + (lane >> 4)));
      }
#pragma unroll
      for (int jn = 0; jn < NI; ++jn) {
        const int gq = lane >> 4, qq = (lane & 15) >> 2, pp = lane & 3;
        const int c = (wn0 + jn * 16) >> 4;
        s16x4 t0, t1;
        {
          unsigned o0 = b_off<BN>(32 * ks + 8 * gq + 0 + qq, 2 * c + (pp >> 1)) + 8 * (pp & 1);
          unsigned o1 = b_off<BN>(32 * ks + 8 * gq + 4 + qq, 2 * c + (pp >> 1)) + 8 * (pp & 1);
          t0 = __builtin_amdgcn_ds_read_tr16_b64_v4i16((__attribute__((address_space(3))) s16x4*)(Bb + o0));
          t1 = __builtin_amdgcn_ds_read_tr16_b64_v4i16((__attribute__((address_space(3))) s16x4*)(Bb + o1));
        }
        bfr[jn] = (s16x8){t0[0], t0[1], t0[2], t0[3], t1[0], t1[1], t1[2], t1[3]};
      }
#pragma unroll
      for (int i = 0; i < MI; ++i)
#pragma unroll
        for (int jn = 0; jn < NI; ++jn)
          acc[i][jn] = __builtin_amdgcn_mfma_f32_16x16x32_bf16(af[i], bfr[jn], acc[i][jn], 0, 0, 0);
    }
    if (kt + 1 < nk) store_tiles(kt + 1, buf ^ 1);
    __syncthreads();
  }

  constexpr int LDC = BN + 4;
  float* Cs = (float*)smem;
#pragma unroll
  for (int i = 0; i < MI; ++i)
#pragma unroll
    for (int jn = 0; jn < NI; ++jn)
#pragma unroll
      for (int r = 0; r < 4; ++r) {
        int row = wm0 + i * 16 + (lane >> 4) * 4 + r;
        int col = wn0 + jn * 16 + (lane & 15);
        Cs[row * LDC + col] = acc[i][jn][r];
      }
  __syncthreads();
  constexpr int CH_PER_ROW = BN / 8;
  constexpr int ITER = BM * CH_PER_ROW / 256;
#pragma unroll
  for (int it = 0; it < ITER; ++it) {
    int c = tid + 256 * it;
    int row = c / CH_PER_ROW, ch = c % CH_PER_ROW;
    int grow = m0 + row, gcol = n0 + ch * 8;
    float v[8];
#pragma unroll
    for (int e = 0; e < 8; ++e) v[e] = rbf(Cs[row * LDC + ch * 8 + e]);
    if (MODE == 1) {
#pragma unroll
      for (int e = 0; e < 8; ++e) v[e] = rbf(gelu_tanh(v[e]));
      u32x4 o;
#pragma unroll
      for (int e = 0; e < 4; ++e) o[e] = pack2(v[2 * e], v[2 * e + 1]);
      *(u32x4*)(g.out + (size_t)grow * F_DIM + gcol) = o;
      if (n0 >= FH_DIM) {
        float sm = 0.f;
#pragma unroll
        for (int e = 0; e < 8; ++e) sm += v[e];
#pragma unroll
        for (int o2 = 8; o2 > 0; o2 >>= 1) sm += __shfl_xor(sm, o2);
        float mean = sm * (1.0f / 128.0f);
        float m2 = 0.f;
#pragma unroll
        for (int e = 0; e < 8; ++e) { float dd = v[e] - mean; m2 += dd * dd; }
#pragma unroll
        for (int o2 = 8; o2 > 0; o2 >>= 1) m2 += __shfl_xor(m2, o2);
        if (ch == 0) g.stats[(size_t)grow * 7 + (n0 - FH_DIM) / 128] = make_float2(mean, m2);
      }
    } else if (MODE == 2) {
      float b = bf2f(g.bsp[grow]);
      u32x4 z1 = *(const u32x4*)(g.z + (size_t)grow * F_DIM + gcol);
      u32x4 o;
#pragma unroll
      for (int e = 0; e < 4; ++e) {
        float g0 = rbf(v[2 * e] + b), g1 = rbf(v[2 * e + 1] + b);
        o[e] = pack2(lo16(z1[e]) * g0, hi16(z1[e]) * g1);
      }
      *(u32x4*)(g.out + (size_t)grow * FH_DIM + gcol) = o;
    } else {
      u32x4 xv = *(const u32x4*)(g.x1 + (size_t)grow * D_DIM + gcol);
      u32x4 o;
#pragma unroll
      for (int e = 0; e < 4; ++e) {
        float gb0 = g.mod[5 * D_DIM + gcol + 2 * e], gb1 = g.mod[5 * D_DIM + gcol + 2 * e + 1];
        o[e] = pack2(lo16(xv[e]) + rbf(gb0 * v[2 * e]), hi16(xv[e]) + rbf(gb1 * v[2 * e + 1]));
      }
      *(u32x4*)(g.out + (size_t)grow * D_DIM + gcol) = o;
    }
  }
}

extern "C" void kernel_launch(void* const* d_in, const int* in_sizes, int n_in, void* d_out, int out_size,
                              void* d_ws, size_t ws_size, hipStream_t stream) {
  const u16* q = (const u16*)d_in[0];
  const u16* k = (const u16*)d_in[1];
  const u16* v = (const u16*)d_in[2];
  const u16* shiftk = (const u16*)d_in[3];
  const u16* ssmk = (const u16*)d_in[4];
  const u16* ssmD = (const u16*)d_in[5];
  const u16* w_in = (const u16*)d_in[7];
  const u16* w_sp = (const u16*)d_in[8];
  const u16* b_sp = (const u16*)d_in[9];
  const u16* w_out = (const u16*)d_in[10];
  const u16* cvec = (const u16*)d_in[11];
  const u16* w_mod = (const u16*)d_in[12];
  u16* out = (u16*)d_out;

  char* ws = (char*)d_ws;
  size_t off = 0;
  float* mod = (float*)(ws + off); off += 16384;
  u16* yT = (u16*)(ws + off); off += (size_t)D_DIM * S_LEN * 2;
  u16* x1 = (u16*)(ws + off); off += (size_t)S_LEN * D_DIM * 2;
  u16* h2 = (u16*)(ws + off); off += (size_t)S_LEN * D_DIM * 2;
  u16* z = (u16*)(ws + off); off += (size_t)S_LEN * F_DIM * 2;
  float2* stats = (float2*)(ws + off); off += (size_t)S_LEN * 7 * 8;
  u16* t = (u16*)(ws + off); off += (size_t)S_LEN * FH_DIM * 2;

  hipFuncSetAttribute((const void*)k1_conv_mod, hipFuncAttributeMaxDynamicSharedMemorySize, 2 * CH_BYTES);
  hipFuncSetAttribute((const void*)gemm_kernel<128, 128, 1>, hipFuncAttributeMaxDynamicSharedMemorySize, 128 * (128 + 4) * 4);
  hipLaunchKernelGGL(k1_conv_mod, dim3(K1_CONV_BLOCKS + K1_MOD_BLOCKS), dim3(512), 2 * CH_BYTES, stream, k, v,
                     shiftk, ssmk, ssmD, cvec, w_mod, mod, yT);
  hipLaunchKernelGGL(k2_rowwise, dim3(S_LEN / 8), dim3(512), 0, stream, q, yT, mod, x1, h2);

  GemmArgs g1;
  g1.A = h2; g1.B = w_in; g1.lda = D_DIM; g1.ldb = F_DIM; g1.K = D_DIM; g1.pad = 0;
  g1.out = z; g1.z = z; g1.stats = stats; g1.bsp = b_sp; g1.x1 = x1; g1.mod = mod;
  hipLaunchKernelGGL((gemm_kernel<128, 128, 1>), dim3(F_DIM / 128, S_LEN / 128), dim3(256),
                     128 * (128 + 4) * 4, stream, g1);

  GemmArgs g2;
  g2.A = w_sp; g2.B = z + FH_DIM; g2.lda = S_LEN; g2.ldb = F_DIM; g2.K = S_LEN; g2.pad = 0;
  g2.out = t; g2.z = z; g2.stats = stats; g2.bsp = b_sp; g2.x1 = x1; g2.mod = mod;
  hipLaunchKernelGGL((gemm_kernel<64, 128, 2>), dim3(FH_DIM / 128, S_LEN / 64), dim3(256),
                     2 * (64 * 64 * 2 + 64 * 128 * 2), stream, g2);

  GemmArgs g3;
  g3.A = t; g3.B = w_out; g3.lda = FH_DIM; g3.ldb = D_DIM; g3.K = FH_DIM; g3.pad = 0;
  g3.out = out; g3.z = z; g3.stats = stats; g3.bsp = b_sp; g3.x1 = x1; g3.mod = mod;
  hipLaunchKernelGGL((gemm_kernel<64, 64, 3>), dim3(D_DIM / 64, S_LEN / 64), dim3(256),
                     2 * (64 * 64 * 2 + 64 * 64 * 2), stream, g3);
}
```

```cpp
#include <hip/hip_runtime.h>
#include <hip/hip_bf16.h>
#include <stdint.h>

#define S_LEN 2048
#define D_DIM 448
#define F_DIM 1792
#define FH_DIM 896
#define MOD_N 2688

typedef unsigned short u16;
typedef __attribute__((ext_vector_type(8))) short s16x8;
typedef __attribute__((ext_vector_type(4))) short s16x4;
typedef __attribute__((ext_vector_type(4))) float f32x4;
typedef __attribute__((ext_vector_type(16))) float f32x16;
typedef __attribute__((ext_vector_type(4))) unsigned u32x4;
typedef __attribute__((ext_vector_type(2))) unsigned u32x2;

__device__ __forceinline__ float bf2f(u16 x) { return __uint_as_float(((unsigned)x) << 16); }
__device__ __forceinline__ u16 f2bf(float f) {
  __hip_bfloat16 b = __float2bfloat16(f);
  return *reinterpret_cast<u16*>(&b);
}
__device__ __forceinline__ float rbf(float f) { return bf2f(f2bf(f)); }
__device__ __forceinline__ float lo16(unsigned u) { return __uint_as_float(u << 16); }
__device__ __forceinline__ float hi16(unsigned u) { return __uint_as_float(u & 0xffff0000u); }
__device__ __forceinline__ unsigned pack2(float a, float b) {
  return (unsigned)f2bf(a) | ((unsigned)f2bf(b) << 16);
}

#define K1_CONV_BLOCKS 224
#define K1_MOD_BLOCKS 21
#define U_BLK_STRIDE 80
#define U_BYTES (96 * U_BLK_STRIDE)
#define KR_STRIDE 4160
#define CH_BYTES 40960
#define KR_OFF 7680
#define RED_OFF 8192

__global__ __launch_bounds__(512, 1) void k1_conv_mod(
    const u16* __restrict__ kin, const u16* __restrict__ vin, const u16* __restrict__ shiftk,
    const u16* __restrict__ ssmk, const u16* __restrict__ ssmD, const u16* __restrict__ cvec,
    const u16* __restrict__ wmod, float* __restrict__ mod_out, u16* __restrict__ yT) {
  extern __shared__ __attribute__((aligned(16))) char smem[];
  const int tid = threadIdx.x;
  const int lane = tid & 63;
  const int wid = tid >> 6;

  if (blockIdx.x >= K1_CONV_BLOCKS) {
    const int cb = blockIdx.x - K1_CONV_BLOCKS;
    float* red = (float*)smem;
    float* sc = (float*)(smem + 8 * 128 * 4);
    if (tid < D_DIM) {
      float x = bf2f(cvec[tid]);
      sc[tid] = rbf(x / (1.0f + __expf(-x)));
    }
    __syncthreads();
    const int col = cb * 128 + lane * 2;
    float a0 = 0.f, a1 = 0.f;
    const int r0 = wid * 56;
#pragma unroll 8
    for (int r = 0; r < 56; ++r) {
      unsigned w = *(const unsigned*)(wmod + (size_t)(r0 + r) * MOD_N + col);
      float s = sc[r0 + r];
      a0 += s * lo16(w);
      a1 += s * hi16(w);
    }
    red[wid * 128 + lane * 2] = a0;
    red[wid * 128 + lane * 2 + 1] = a1;
    __syncthreads();
    if (tid < 128) {
      float s = 0.f;
#pragma unroll
      for (int w = 0; w < 8; ++w) s += red[w * 128 + tid];
      mod_out[cb * 128 + tid] = rbf(s);
    }
    return;
  }

  const int d0 = blockIdx.x * 2;
  for (int i = tid; i < 2 * 32 * U_BLK_STRIDE / 4; i += 512) {
    int ch = i / (32 * U_BLK_STRIDE / 4);
    int o = i % (32 * U_BLK_STRIDE / 4);
    *(unsigned*)(smem + ch * CH_BYTES + o * 4) = 0u;
  }
  {
    const int ch = tid >> 8;
    const int t = tid & 255;
    u32x4 kk = *(const u32x4*)(ssmk + (size_t)(d0 + ch) * S_LEN + t * 8);
    char* kr = smem + ch * CH_BYTES + KR_OFF;
    u16 taps[8];
#pragma unroll
    for (int e = 0; e < 4; ++e) { taps[2 * e] = (u16)(kk[e] & 0xffff); taps[2 * e + 1] = (u16)(kk[e] >> 16); }
#pragma unroll
    for (int e = 0; e < 8; ++e) {
      int p = 2047 - (t * 8 + e);
#pragma unroll
      for (int s = 0; s < 4; ++s) {
        int q = p - s;
        if (q >= 0) *(u16*)(kr + s * KR_STRIDE + q * 2) = taps[e];
      }
    }
    if (tid < 2 * 4 * 32) {
      int c2 = tid >> 7, s = (tid >> 5) & 3, e = tid & 31;
      int q = 2048 + e - s;
      *(u16*)(smem + c2 * CH_BYTES + KR_OFF + s * KR_STRIDE + q * 2) = 0;
    }
  }
  {
    const int s0 = tid * 4;
    unsigned kr[4], vr[4], kh[3];
#pragma unroll
    for (int i = 0; i < 4; ++i) {
      kr[i] = *(const unsigned*)(kin + (size_t)(s0 + i) * D_DIM + d0);
      vr[i] = *(const unsigned*)(vin + (size_t)(s0 + i) * D_DIM + d0);
    }
#pragma unroll
    for (int i = 0; i < 3; ++i) {
      unsigned fromprev = __shfl_up(kr[i + 1], 1);
      unsigned g = 0u;
      if (lane == 0) {
        int s = s0 - 3 + i;
        if (s >= 0) g = *(const unsigned*)(kin + (size_t)s * D_DIM + d0);
      }
      kh[i] = (lane == 0) ? g : fromprev;
    }
    float sk0[4], sk1[4];
#pragma unroll
    for (int j = 0; j < 4; ++j) {
      sk0[j] = bf2f(shiftk[(d0)*4 + j]);
      sk1[j] = bf2f(shiftk[(d0 + 1) * 4 + j]);
    }
    unsigned kall[7] = {kh[0], kh[1], kh[2], kr[0], kr[1], kr[2], kr[3]};
#pragma unroll
    for (int i = 0; i < 4; ++i) {
      float a0 = 0.f, a1 = 0.f;
#pragma unroll
      for (int t = 0; t < 4; ++t) {
        a0 = rbf(a0 + rbf(lo16(kall[i + t]) * sk0[3 - t]));
        a1 = rbf(a1 + rbf(hi16(kall[i + t]) * sk1[3 - t]));
      }
      float kv0 = rbf(a0 * lo16(vr[i]));
      float kv1 = rbf(a1 * hi16(vr[i]));
      int s = s0 + i;
      int off = (32 + (s >> 5)) * U_BLK_STRIDE + (s & 31) * 2;
      *(u16*)(smem + off) = f2bf(kv0);
      *(u16*)(smem + CH_BYTES + off) = f2bf(kv1);
    }
  }
  __syncthreads();

  const int ch = wid >> 2;
  const int wq = wid & 3;
  const char* U = smem + ch * CH_BYTES;
  const char* KR = smem + ch * CH_BYTES + KR_OFF;
  const int j = lane & 31;
  const int kg = lane >> 5;
  f32x16 acc0, acc1;
#pragma unroll
  for (int i = 0; i < 16; ++i) { acc0[i] = 0.f; acc1[i] = 0.f; }
  for (int dl = wq; dl < 64; dl += 4) {
#pragma unroll
    for (int h = 0; h < 2; ++h) {
      int n = 32 * dl + j - 16 * h - 8 * kg;
      int p0 = 2047 - n;
      int s = p0 & 3;
      int q = p0 - s;
      const char* ap = KR + s * KR_STRIDE + q * 2;
      u32x2 a_lo = *(const u32x2*)(ap);
      u32x2 a_hi = *(const u32x2*)(ap + 8);
      u32x4 au = {a_lo[0], a_lo[1], a_hi[0], a_hi[1]};
      s16x8 afrag = __builtin_bit_cast(s16x8, au);
      {
        int blk = 32 + (32 + j - dl);
        u32x4 bu = *(const u32x4*)(U + blk * U_BLK_STRIDE + (16 * h + 8 * kg) * 2);
        acc1 = __builtin_amdgcn_mfma_f32_32x32x16_bf16(afrag, __builtin_bit_cast(s16x8, bu), acc1, 0, 0, 0);
      }
      if (dl < 32) {
        int blk = 32 + (j - dl);
        u32x4 bu = *(const u32x4*)(U + blk * U_BLK_STRIDE + (16 * h + 8 * kg) * 2);
        acc0 = __builtin_amdgcn_mfma_f32_32x32x16_bf16(afrag, __builtin_bit_cast(s16x8, bu), acc0, 0, 0, 0);
      }
    }
  }
  __syncthreads();
  {
    float* red = (float*)(smem + ch * CH_BYTES + RED_OFF);
#pragma unroll
    for (int r = 0; r < 16; ++r) {
      int row = (r & 3) + 8 * (r >> 2) + 4 * kg;
      red[((wq * 2 + 0) * 32 + row) * 32 + j] = acc0[r];
      red[((wq * 2 + 1) * 32 + row) * 32 + j] = acc1[r];
    }
  }
  __syncthreads();
  {
    const int c2 = tid >> 8;
    const int t = tid & 255;
    const int sbase = t * 8;
    const float* red = (const float*)(smem + c2 * CH_BYTES + RED_OFF);
    const char* Uc = smem + c2 * CH_BYTES;
    const float Dv = bf2f(ssmD[d0 + c2]);
    u16 outv[8];
#pragma unroll
    for (int e = 0; e < 8; ++e) {
      int s = sbase + e;
      int i = s >> 5, jj = s & 31;
      int tile = i >> 5, col = i & 31;
      float sum = 0.f;
#pragma unroll
      for (int w = 0; w < 4; ++w) sum += red[((w * 2 + tile) * 32 + jj) * 32 + col];
      float y = rbf(sum);
      float kv = bf2f(*(const u16*)(Uc + (32 + i) * U_BLK_STRIDE + jj * 2));
      y = rbf(y + rbf(kv * Dv));
      outv[e] = f2bf(y);
    }
    u32x4 o;
#pragma unroll
    for (int e = 0; e < 4; ++e) o[e] = (unsigned)outv[2 * e] | ((unsigned)outv[2 * e + 1] << 16);
    *(u32x4*)(yT + (size_t)(d0 + c2) * S_LEN + sbase) = o;
  }
}

__device__ __forceinline__ float wave_sum(float v) {
#pragma unroll
  for (int o = 32; o > 0; o >>= 1) v += __shfl_xor(v, o);
  return v;
}

__global__ __launch_bounds__(512) void k2_rowwise(const u16* __restrict__ q, const u16* __restrict__ yT,
                                                  const float* __restrict__ mod, u16* __restrict__ x1out,
                                                  u16* __restrict__ h2out) {
  __shared__ __attribute__((aligned(16))) u16 ys[8][D_DIM + 8];
  const int tid = threadIdx.x;
  const int lane = tid & 63;
  const int wid = tid >> 6;
  const int s0 = blockIdx.x * 8;
  if (tid < D_DIM) {
    u32x4 v = *(const u32x4*)(yT + (size_t)tid * S_LEN + s0);
#pragma unroll
    for (int e = 0; e < 4; ++e) {
      ys[2 * e][tid] = (u16)(v[e] & 0xffff);
      ys[2 * e + 1][tid] = (u16)(v[e] >> 16);
    }
  }
  __syncthreads();
  const int s = s0 + wid;
  const bool act = lane < 56;
  const int c0 = lane * 8;
  float x[8], yv[8];
  float sum = 0.f;
  if (act) {
    u32x4 qv = *(const u32x4*)(q + (size_t)s * D_DIM + c0);
    u32x4 yy = *(const u32x4*)(&ys[wid][c0]);
#pragma unroll
    for (int e = 0; e < 4; ++e) {
      x[2 * e] = lo16(qv[e]); x[2 * e + 1] = hi16(qv[e]);
      yv[2 * e] = lo16(yy[e]); yv[2 * e + 1] = hi16(yy[e]);
    }
#pragma unroll
    for (int e = 0; e < 8; ++e) sum += x[e];
  } else {
#pragma unroll
    for (int e = 0; e < 8; ++e) { x[e] = 0.f; yv[e] = 0.f; }
  }
  float mean = wave_sum(sum) * (1.0f / D_DIM);
  float sq = 0.f;
  if (act) {
#pragma unroll
    for (int e = 0; e < 8; ++e) { float dd = x[e] - mean; sq += dd * dd; }
  }
  float var = wave_sum(sq) * (1.0f / D_DIM);
  float m_b = rbf(mean);
  float sd = rbf(sqrtf(rbf(rbf(var) + 1e-5f)));
  float x1[8];
  float sum2 = 0.f;
  if (act) {
#pragma unroll
    for (int e = 0; e < 8; ++e) {
      int c = c0 + e;
      float sa = mod[0 * D_DIM + c], ca = mod[1 * D_DIM + c], ga = mod[2 * D_DIM + c];
      float n = rbf(rbf(x[e] - m_b) / sd);
      float h = rbf(rbf(n * rbf(1.0f + ca)) + sa);
      float a = rbf(h * yv[e]);
      x1[e] = rbf(x[e] + rbf(ga * a));
      sum2 += x1[e];
    }
  } else {
#pragma unroll
    for (int e = 0; e < 8; ++e) x1[e] = 0.f;
  }
  float mean2 = wave_sum(sum2) * (1.0f / D_DIM);
  float sq2 = 0.f;
  if (act) {
#pragma unroll
    for (int e = 0; e < 8; ++e) { float dd = x1[e] - mean2; sq2 += dd * dd; }
  }
  float var2 = wave_sum(sq2) * (1.0f / D_DIM);
  float m2_b = rbf(mean2);
  float sd2 = rbf(sqrtf(rbf(rbf(var2) + 1e-5f)));
  if (act) {
    u32x4 xo, ho;
    float hh[8];
#pragma unroll
    for (int e = 0; e < 8; ++e) {
      int c = c0 + e;
      float sb = mod[3 * D_DIM + c], cb = mod[4 * D_DIM + c];
      float n = rbf(rbf(x1[e] - m2_b) / sd2);
      hh[e] = rbf(rbf(n * rbf(1.0f + cb)) + sb);
    }
#pragma unroll
    for (int e = 0; e < 4; ++e) {
      xo[e] = pack2(x1[2 * e], x1[2 * e + 1]);
      ho[e] = pack2(hh[2 * e], hh[2 * e + 1]);
    }
    *(u32x4*)(x1out + (size_t)s * D_DIM + c0) = xo;
    *(u32x4*)(h2out + (size_t)s * D_DIM + c0) = ho;
  }
}

struct GemmArgs {
  const u16* A;
  const u16* B;
  int lda, ldb;
  int K, pad;
  u16* out;
  const u16* z;
  float2* stats;
  const u16* bsp;
  const u16* x1;
  const float* mod;
};

__device__ __forceinline__ int a_swz(int row) { return row & 7; }
template <int BN>
__device__ __forceinline__ int b_swz(int row) {
  if (BN == 128) return ((row & 3) << 2) | ((row >> 2) & 3);
  else return (((row >> 1) & 1) << 1) | (((row >> 3) & 1) << 2);
}
__device__ __forceinline__ unsigned a_off(int row, int ch) { return (unsigned)(row * 128 + 16 * (ch ^ a_swz(row))); }
template <int BN>
__device__ __forceinline__ unsigned b_off(int row, int ch) { return (unsigned)(BN * 2 * row + 16 * (ch ^ b_swz<BN>(row))); }

__device__ __forceinline__ float gelu_tanh(float x) {
  const float k0 = 0.7978845608028654f, k1 = 0.044715f;
  float u = k0 * (x + k1 * x * x * x);
  float t = 1.0f - 2.0f / (1.0f + __expf(2.0f * u));
  return 0.5f * x * (1.0f + t);
}

template <int BM, int BN, int MODE>
__global__ __launch_bounds__(256) void gemm_kernel(GemmArgs g) {
  constexpr int BK = 64;
  constexpr int NST = 3;
  constexpr int WM = BM / 2, WN = BN / 2;
  constexpr int MI = WM / 16, NI = WN / 16;
  constexpr int A_BYTES = BM * BK * 2;
  constexpr int B_BYTES = BK * BN * 2;
  constexpr int ST_BYTES = A_BYTES + B_BYTES;
  constexpr int A_INS = A_BYTES / 4096;
  constexpr int B_INS = B_BYTES / 4096;
  constexpr int CPR = BN / 8;
  extern __shared__ __attribute__((aligned(16))) char smem[];

  const int tid = threadIdx.x;
  const int lane = tid & 63;
  const int wid = __builtin_amdgcn_readfirstlane(tid >> 6);
  const int wm0 = (wid >> 1) * WM;
  const int wn0 = (wid & 1) * WN;
  const int m0 = blockIdx.y * BM;
  const int n0 = blockIdx.x * BN;
  const int nk = g.K / BK;

  f32x4 acc[MI][NI];
#pragma unroll
  for (int i = 0; i < MI; ++i)
#pragma unroll
    for (int jn = 0; jn < NI; ++jn) acc[i][jn] = (f32x4){0.f, 0.f, 0.f, 0.f};

  const u16* a_src[A_INS];
  const u16* b_src[B_INS];
#pragma unroll
  for (int i = 0; i < A_INS; ++i) {
    int L = (wid * A_INS + i) * 64 + lane;
    int row = L >> 3, chp = L & 7;
    int ch = chp ^ a_swz(row);
    a_src[i] = g.A + (size_t)(m0 + row) * g.lda + ch * 8;
  }
#pragma unroll
  for (int i = 0; i < B_INS; ++i) {
    int L = (wid * B_INS + i) * 64 + lane;
    int row = L / CPR, chp = L % CPR;
    int ch = chp ^ b_swz<BN>(row);
    b_src[i] = g.B + (size_t)row * g.ldb + n0 + ch * 8;
  }
  auto issue = [&](int kt) {
    const int st = kt % NST;
    char* As = smem + st * ST_BYTES;
    char* Bs = As + A_BYTES;
#pragma unroll
    for (int i = 0; i < A_INS; ++i)
      __builtin_amdgcn_global_load_lds((const void*)(a_src[i] + kt * BK),
                                       (__attribute__((address_space(3))) void*)(As + (wid * A_INS + i) * 1024), 16, 0, 0);
#pragma unroll
    for (int i = 0; i < B_INS; ++i)
      __builtin_amdgcn_global_load_lds((const void*)(b_src[i] + (size_t)kt * BK * g.ldb),
                                       (__attribute__((address_space(3))) void*)(Bs + (wid * B_INS + i) * 1024), 16, 0, 0);
  };

  issue(0);
  if (nk > 1) issue(1);
  for (int kt = 0; kt < nk; ++kt) {
    if (kt + 2 < nk) {
      issue(kt + 2);
      asm volatile("s_waitcnt vmcnt(%0)" ::"n"(2 * (A_INS + B_INS)) : "memory");
    } else if (kt + 1 < nk) {
      asm volatile("s_waitcnt vmcnt(%0)" ::"n"(A_INS + B_INS) : "memory");
    } else {
      asm volatile("s_waitcnt vmcnt(0)" ::: "memory");
    }
    __builtin_amdgcn_s_barrier();
    const int st = kt % NST;
    const char* Ab = smem + st * ST_BYTES;
    const char* Bb = Ab + A_BYTES;
#pragma unroll
    for (int ks = 0; ks < 2; ++ks) {
      s16x8 af[MI], bfr[NI];
#pragma unroll
      for (int i = 0; i < MI; ++i) {
        int row = wm0 + i * 16 + (lane & 15);
        af[i] = *(const s16x8*)(Ab + a_off(row, ks * 4 + (lane >> 4)));
      }
#pragma unroll
      for (int jn = 0; jn < NI; ++jn) {
        const int gq = lane >> 4, qq = (lane & 15) >> 2, pp = lane & 3;
        const int c = (wn0 + jn * 16) >> 4;
        unsigned o0 = b_off<BN>(32 * ks + 8 * gq + 0 + qq, 2 * c + (pp >> 1)) + 8 * (pp & 1);
        unsigned o1 = b_off<BN>(32 * ks + 8 * gq + 4 + qq, 2 * c + (pp >> 1)) + 8 * (pp & 1);
        s16x4 t0 = __builtin_amdgcn_ds_read_tr16_b64_v4i16((__attribute__((address_space(3))) s16x4*)(Bb + o0));
        s16x4 t1 = __builtin_amdgcn_ds_read_tr16_b64_v4i16((__attribute__((address_space(3))) s16x4*)(Bb + o1));
        bfr[jn] = (s16x8){t0[0], t0[1], t0[2], t0[3], t1[0], t1[1], t1[2], t1[3]};
      }
#pragma unroll
      for (int i = 0; i < MI; ++i)
#pragma unroll
        for (int jn = 0; jn < NI; ++jn)
          acc[i][jn] = __builtin_amdgcn_mfma_f32_16x16x32_bf16(af[i], bfr[jn], acc[i][jn], 0, 0, 0);
    }
    asm volatile("s_waitcnt lgkmcnt(0)" ::: "memory");
    __builtin_amdgcn_s_barrier();
  }

  constexpr int LDC = BN + 4;
  float* Cs = (float*)smem;
#pragma unroll
  for (int i = 0; i < MI; ++i)
#pragma unroll
    for (int jn = 0; jn < NI; ++jn)
#pragma unroll
      for (int r = 0; r < 4; ++r) {
        int row = wm0 + i * 16 + (lane >> 4) * 4 + r;
        int col = wn0 + jn * 16 + (lane & 15);
        Cs[row * LDC + col] = acc[i][jn][r];
      }
  __syncthreads();
  constexpr int CH_PER_ROW = BN / 8;
  constexpr int ITER = BM * CH_PER_ROW / 256;
#pragma unroll
  for (int it = 0; it < ITER; ++it) {
    int c = tid + 256 * it;
    int row = c / CH_PER_ROW, ch = c % CH_PER_ROW;
    int grow = m0 + row, gcol = n0 + ch * 8;
    float v[8];
    f32x4 c0 = *(const f32x4*)(&Cs[row * LDC + ch * 8]);
    f32x4 c1 = *(const f32x4*)(&Cs[row * LDC + ch * 8 + 4]);
#pragma unroll
    for (int e = 0; e < 4; ++e) { v[e] = rbf(c0[e]); v[4 + e] = rbf(c1[e]); }
    if (MODE == 1) {
#pragma unroll
      for (int e = 0; e < 8; ++e) v[e] = rbf(gelu_tanh(v[e]));
      u32x4 o;
#pragma unroll
      for (int e = 0; e < 4; ++e) o[e] = pack2(v[2 * e], v[2 * e + 1]);
      *(u32x4*)(g.out + (size_t)grow * F_DIM + gcol) = o;
      if (n0 >= FH_DIM) {
        float sm = 0.f;
#pragma unroll
        for (int e = 0; e < 8; ++e) sm += v[e];
#pragma unroll
        for (int o2 = 8; o2 > 0; o2 >>= 1) sm += __shfl_xor(sm, o2);
        float mean = sm * (1.0f / 128.0f);
        float m2 = 0.f;
#pragma unroll
        for (int e = 0; e < 8; ++e) { float dd = v[e] - mean; m2 += dd * dd; }
#pragma unroll
        for (int o2 = 8; o2 > 0; o2 >>= 1) m2 += __shfl_xor(m2, o2);
        if (ch == 0) g.stats[(size_t)grow * 7 + (n0 - FH_DIM) / 128] = make_float2(mean, m2);
      }
    } else if (MODE == 2) {
      float b = bf2f(g.bsp[grow]);
      u32x4 z1 = *(const u32x4*)(g.z + (size_t)grow * F_DIM + gcol);
      u32x4 o;
#pragma unroll
      for (int e = 0; e < 4; ++e) {
        float g0 = rbf(v[2 * e] + b), g1 = rbf(v[2 * e + 1] + b);
        o[e] = pack2(lo16(z1[e]) * g0, hi16(z1[e]) * g1);
      }
      *(u32x4*)(g.out + (size_t)grow * FH_DIM + gcol) = o;
    } else {
      u32x4 xv = *(const u32x4*)(g.x1 + (size_t)grow * D_DIM + gcol);
      u32x4 o;
#pragma unroll
      for (int e = 0; e < 4; ++e) {
        float gb0 = g.mod[5 * D_DIM + gcol + 2 * e], gb1 = g.mod[5 * D_DIM + gcol + 2 * e + 1];
        o[e] = pack2(lo16(xv[e]) + rbf(gb0 * v[2 * e]), hi16(xv[e]) + rbf(gb1 * v[2 * e + 1]));
      }
      *(u32x4*)(g.out + (size_t)grow * D_DIM + gcol) = o;
    }
  }
}

__global__ __launch_bounds__(256) void k3b_ln_z2(const u16* __restrict__ z, const float2* __restrict__ stats,
                                                 u16* __restrict__ z2n) {
  const int row = blockIdx.x * 4 + (threadIdx.x >> 6);
  const int lane = threadIdx.x & 63;
  const float2* st = stats + (size_t)row * 7;
  float mean = 0.f;
#pragma unroll
  for (int b = 0; b < 7; ++b) mean += st[b].x;
  mean *= (1.0f / 7.0f);
  float m2 = 0.f;
#pragma unroll
  for (int b = 0; b < 7; ++b) { float dd = st[b].x - mean; m2 += st[b].y + 128.0f * dd * dd; }
  float var = m2 * (1.0f / FH_DIM);
  float mb = rbf(mean);
  float sd = rbf(sqrtf(rbf(rbf(var) + 1e-5f)));
#pragma unroll
  for (int i = 0; i < 2; ++i) {
    int c = lane + 64 * i;
    if (c < 112) {
      u32x4 v = *(const u32x4*)(z + (size_t)row * F_DIM + FH_DIM + c * 8);
#pragma unroll
      for (int e = 0; e < 4; ++e) {
        float a = rbf(rbf(lo16(v[e]) - mb) / sd);
        float b2 = rbf(rbf(hi16(v[e]) - mb) / sd);
        v[e] = pack2(a, b2);
      }
      *(u32x4*)(z2n + (size_t)row * FH_DIM + c * 8) = v;
    }
  }
}

extern "C" void kernel_launch(void* const* d_in, const int* in_sizes, int n_in, void* d_out, int out_size,
                              void* d_ws, size_t ws_size, hipStream_t stream) {
  const u16* q = (const u16*)d_in[0];
  const u16* k = (const u16*)d_in[1];
  const u16* v = (const u16*)d_in[2];
  const u16* shiftk = (const u16*)d_in[3];
  const u16* ssmk = (const u16*)d_in[4];
  const u16* ssmD = (const u16*)d_in[5];
  const u16* w_in = (const u16*)d_in[7];
  const u16* w_sp = (const u16*)d_in[8];
  const u16* b_sp = (const u16*)d_in[9];
  const u16* w_out = (const u16*)d_in[10];
  const u16* cvec = (const u16*)d_in[11];
  const u16* w_mod = (const u16*)d_in[12];
  u16* out = (u16*)d_out;

  char* ws = (char*)d_ws;
  size_t off = 0;
  float* mod = (float*)(ws + off); off += 16384;
  u16* yT = (u16*)(ws + off); off += (size_t)D_DIM * S_LEN * 2;
  u16* x1 = (u16*)(ws + off); off += (size_t)S_LEN * D_DIM * 2;
  u16* h2 = (u16*)(ws + off); off += (size_t)S_LEN * D_DIM * 2;
  u16* z = (u16*)(ws + off); off += (size_t)S_LEN * F_DIM * 2;
  float2* stats = (float2*)(ws + off); off += (size_t)S_LEN * 7 * 8;
  u16* t = (u16*)(ws + off); off += (size_t)S_LEN * FH_DIM * 2;
  u16* z2n = (u16*)(ws + off); off += (size_t)S_LEN * FH_DIM * 2;

  hipFuncSetAttribute((const void*)k1_conv_mod, hipFuncAttributeMaxDynamicSharedMemorySize, 2 * CH_BYTES);
  hipFuncSetAttribute((const void*)gemm_kernel<128, 128, 1>, hipFuncAttributeMaxDynamicSharedMemorySize, 98304);
  hipFuncSetAttribute((const void*)gemm_kernel<128, 64, 2>, hipFuncAttributeMaxDynamicSharedMemorySize, 73728);
  hipLaunchKernelGGL(k1_conv_mod, dim3(K1_CONV_BLOCKS + K1_MOD_BLOCKS), dim3(512), 2 * CH_BYTES, stream, k, v,
                     shiftk, ssmk, ssmD, cvec, w_mod, mod, yT);
  hipLaunchKernelGGL(k2_rowwise, dim3(S_LEN / 8), dim3(512), 0, stream, q, yT, mod, x1, h2);

  GemmArgs g1;
  g1.A = h2; g1.B = w_in; g1.lda = D_DIM; g1.ldb = F_DIM; g1.K = D_DIM; g1.pad = 0;
  g1.out = z; g1.z = z; g1.stats = stats; g1.bsp = b_sp; g1.x1 = x1; g1.mod = mod;
  hipLaunchKernelGGL((gemm_kernel<128, 128, 1>), dim3(F_DIM / 128, S_LEN / 128), dim3(256),
                     3 * (128 * 64 * 2 + 64 * 128 * 2), stream, g1);

  hipLaunchKernelGGL(k3b_ln_z2, dim3(S_LEN / 4), dim3(256), 0, stream, z, stats, z2n);

  GemmArgs g2;
  g2.A = w_sp; g2.B = z2n; g2.lda = S_LEN; g2.ldb = FH_DIM; g2.K = S_LEN; g2.pad = 0;
  g2.out = t; g2.z = z; g2.stats = stats; g2.bsp = b_sp; g2.x1 = x1; g2.mod = mod;
  hipLaunchKernelGGL((gemm_kernel<128, 64, 2>), dim3(FH_DIM / 64, S_LEN / 128), dim3(256),
                     3 * (128 * 64 * 2 + 64 * 64 * 2), stream, g2);

  GemmArgs g3;
  g3.A = t; g3.B = w_out; g3.lda = FH_DIM; g3.ldb = D_DIM; g3.K = FH_DIM; g3.pad = 0;
  g3.out = out; g3.z = z; g3.stats = stats; g3.bsp = b_sp; g3.x1 = x1; g3.mod = mod;
  hipLaunchKernelGGL((gemm_kernel<64, 64, 3>), dim3(D_DIM / 64, S_LEN / 64), dim3(256),
                     3 * (64 * 64 * 2 + 64 * 64 * 2), stream, g3);
}
```

```cpp
#include <hip/hip_runtime.h>
#include <hip/hip_bf16.h>
#include <stdint.h>

#define S_LEN 2048
#define D_DIM 448
#define F_DIM 1792
#define FH_DIM 896
#define MOD_N 2688

typedef unsigned short u16;
typedef __attribute__((ext_vector_type(8))) short s16x8;
typedef __attribute__((ext_vector_type(4))) short s16x4;
typedef __attribute__((ext_vector_type(4))) float f32x4;
typedef __attribute__((ext_vector_type(16))) float f32x16;
typedef __attribute__((ext_vector_type(4))) unsigned u32x4;
typedef __attribute__((ext_vector_type(2))) unsigned u32x2;

__device__ __forceinline__ float bf2f(u16 x) { return __uint_as_float(((unsigned)x) << 16); }
__device__ __forceinline__ u16 f2bf(float f) {
  __hip_bfloat16 b = __float2bfloat16(f);
  return *reinterpret_cast<u16*>(&b);
}
__device__ __forceinline__ float rbf(float f) { return bf2f(f2bf(f)); }
__device__ __forceinline__ float lo16(unsigned u) { return __uint_as_float(u << 16); }
__device__ __forceinline__ float hi16(unsigned u) { return __uint_as_float(u & 0xffff0000u); }
__device__ __forceinline__ unsigned pack2(float a, float b) {
  return (unsigned)f2bf(a) | ((unsigned)f2bf(b) << 16);
}

#define K1_CONV_BLOCKS 224
#define K1_MOD_BLOCKS 21
#define U_BLK_STRIDE 80
#define U_BYTES (96 * U_BLK_STRIDE)
#define KR_STRIDE 4160
#define CH_BYTES 40960
#define KR_OFF 7680
#define RED_OFF 8192

__global__ __launch_bounds__(512, 1) void k1_conv_mod(
    const u16* __restrict__ kin, const u16* __restrict__ vin, const u16* __restrict__ shiftk,
    const u16* __restrict__ ssmk, const u16* __restrict__ ssmD, const u16* __restrict__ cvec,
    const u16* __restrict__ wmod, float* __restrict__ mod_out, u16* __restrict__ yT) {
  extern __shared__ __attribute__((aligned(16))) char smem[];
  const int tid = threadIdx.x;
  const int lane = tid & 63;
  const int wid = tid >> 6;

  if (blockIdx.x >= K1_CONV_BLOCKS) {
    const int cb = blockIdx.x - K1_CONV_BLOCKS;
    float* red = (float*)smem;
    float* sc = (float*)(smem + 8 * 128 * 4);
    if (tid < D_DIM) {
      float x = bf2f(cvec[tid]);
      sc[tid] = rbf(x / (1.0f + __expf(-x)));
    }
    __syncthreads();
    const int col = cb * 128 + lane * 2;
    float a0 = 0.f, a1 = 0.f;
    const int r0 = wid * 56;
#pragma unroll 8
    for (int r = 0; r < 56; ++r) {
      unsigned w = *(const unsigned*)(wmod + (size_t)(r0 + r) * MOD_N + col);
      float s = sc[r0 + r];
      a0 += s * lo16(w);
      a1 += s * hi16(w);
    }
    red[wid * 128 + lane * 2] = a0;
    red[wid * 128 + lane * 2 + 1] = a1;
    __syncthreads();
    if (tid < 128) {
      float s = 0.f;
#pragma unroll
      for (int w = 0; w < 8; ++w) s += red[w * 128 + tid];
      mod_out[cb * 128 + tid] = rbf(s);
    }
    return;
  }

  const int d0 = blockIdx.x * 2;
  for (int i = tid; i < 2 * 32 * U_BLK_STRIDE / 4; i += 512) {
    int ch = i / (32 * U_BLK_STRIDE / 4);
    int o = i % (32 * U_BLK_STRIDE / 4);
    *(unsigned*)(smem + ch * CH_BYTES + o * 4) = 0u;
  }
  {
    const int ch = tid >> 8;
    const int t = tid & 255;
    u32x4 kk = *(const u32x4*)(ssmk + (size_t)(d0 + ch) * S_LEN + t * 8);
    char* kr = smem + ch * CH_BYTES + KR_OFF;
    u16 taps[8];
#pragma unroll
    for (int e = 0; e < 4; ++e) { taps[2 * e] = (u16)(kk[e] & 0xffff); taps[2 * e + 1] = (u16)(kk[e] >> 16); }
#pragma unroll
    for (int e = 0; e < 8; ++e) {
      int p = 2047 - (t * 8 + e);
#pragma unroll
      for (int s = 0; s < 4; ++s) {
        int q = p - s;
        if (q >= 0) *(u16*)(kr + s * KR_STRIDE + q * 2) = taps[e];
      }
    }
    if (tid < 2 * 4 * 32) {
      int c2 = tid >> 7, s = (tid >> 5) & 3, e = tid & 31;
      int q = 2048 + e - s;
      *(u16*)(smem + c2 * CH_BYTES + KR_OFF + s * KR_STRIDE + q * 2) = 0;
    }
  }
  {
    const int s0 = tid * 4;
    unsigned kr[4], vr[4], kh[3];
#pragma unroll
    for (int i = 0; i < 4; ++i) {
      kr[i] = *(const unsigned*)(kin + (size_t)(s0 + i) * D_DIM + d0);
      vr[i] = *(const unsigned*)(vin + (size_t)(s0 + i) * D_DIM + d0);
    }
#pragma unroll
    for (int i = 0; i < 3; ++i) {
      unsigned fromprev = __shfl_up(kr[i + 1], 1);
      unsigned g = 0u;
      if (lane == 0) {
        int s = s0 - 3 + i;
        if (s >= 0) g = *(const unsigned*)(kin + (size_t)s * D_DIM + d0);
      }
      kh[i] = (lane == 0) ? g : fromprev;
    }
    float sk0[4], sk1[4];
#pragma unroll
    for (int j = 0; j < 4; ++j) {
      sk0[j] = bf2f(shiftk[(d0)*4 + j]);
      sk1[j] = bf2f(shiftk[(d0 + 1) * 4 + j]);
    }
    unsigned kall[7] = {kh[0], kh[1], kh[2], kr[0], kr[1], kr[2], kr[3]};
#pragma unroll
    for (int i = 0; i < 4; ++i) {
      float a0 = 0.f, a1 = 0.f;
#pragma unroll
      for (int t = 0; t < 4; ++t) {
        a0 = rbf(a0 + rbf(lo16(kall[i + t]) * sk0[3 - t]));
        a1 = rbf(a1 + rbf(hi16(kall[i + t]) * sk1[3 - t]));
      }
      float kv0 = rbf(a0 * lo16(vr[i]));
      float kv1 = rbf(a1 * hi16(vr[i]));
      int s = s0 + i;
      int off = (32 + (s >> 5)) * U_BLK_STRIDE + (s & 31) * 2;
      *(u16*)(smem + off) = f2bf(kv0);
      *(u16*)(smem + CH_BYTES + off) = f2bf(kv1);
    }
  }
  __syncthreads();

  const int ch = wid >> 2;
  const int wq = wid & 3;
  const char* U = smem + ch * CH_BYTES;
  const char* KR = smem + ch * CH_BYTES + KR_OFF;
  const int j = lane & 31;
  const int kg = lane >> 5;
  f32x16 acc0, acc1;
#pragma unroll
  for (int i = 0; i < 16; ++i) { acc0[i] = 0.f; acc1[i] = 0.f; }
  for (int dl = wq; dl < 64; dl += 4) {
#pragma unroll
    for (int h = 0; h < 2; ++h) {
      int n = 32 * dl + j - 16 * h - 8 * kg;
      int p0 = 2047 - n;
      int s = p0 & 3;
      int q = p0 - s;
      const char* ap = KR + s * KR_STRIDE + q * 2;
      u32x2 a_lo = *(const u32x2*)(ap);
      u32x2 a_hi = *(const u32x2*)(ap + 8);
      u32x4 au = {a_lo[0], a_lo[1], a_hi[0], a_hi[1]};
      s16x8 afrag = __builtin_bit_cast(s16x8, au);
      {
        int blk = 32 + (32 + j - dl);
        u32x4 bu = *(const u32x4*)(U + blk * U_BLK_STRIDE + (16 * h + 8 * kg) * 2);
        acc1 = __builtin_amdgcn_mfma_f32_32x32x16_bf16(afrag, __builtin_bit_cast(s16x8, bu), acc1, 0, 0, 0);
      }
      if (dl < 32) {
        int blk = 32 + (j - dl);
        u32x4 bu = *(const u32x4*)(U + blk * U_BLK_STRIDE + (16 * h + 8 * kg) * 2);
        acc0 = __builtin_amdgcn_mfma_f32_32x32x16_bf16(afrag, __builtin_bit_cast(s16x8, bu), acc0, 0, 0, 0);
      }
    }
  }
  __syncthreads();
  {
    float* red = (float*)(smem + ch * CH_BYTES + RED_OFF);
#pragma unroll
    for (int r = 0; r < 16; ++r) {
      int row = (r & 3) + 8 * (r >> 2) + 4 * kg;
      red[((wq * 2 + 0) * 32 + row) * 32 + j] = acc0[r];
      red[((wq * 2 + 1) * 32 + row) * 32 + j] = acc1[r];
    }
  }
  __syncthreads();
  {
    const int c2 = tid >> 8;
    const int t = tid & 255;
    const int sbase = t * 8;
    const float* red = (const float*)(smem + c2 * CH_BYTES + RED_OFF);
    const char* Uc = smem + c2 * CH_BYTES;
    const float Dv = bf2f(ssmD[d0 + c2]);
    u16 outv[8];
#pragma unroll
    for (int e = 0; e < 8; ++e) {
      int s = sbase + e;
      int i = s >> 5, jj = s & 31;
      int tile = i >> 5, col = i & 31;
      float sum = 0.f;
#pragma unroll
      for (int w = 0; w < 4; ++w) sum += red[((w * 2 + tile) * 32 + jj) * 32 + col];
      float y = rbf(sum);
      float kv = bf2f(*(const u16*)(Uc + (32 + i) * U_BLK_STRIDE + jj * 2));
      y = rbf(y + rbf(kv * Dv));
      outv[e] = f2bf(y);
    }
    u32x4 o;
#pragma unroll
    for (int e = 0; e < 4; ++e) o[e] = (unsigned)outv[2 * e] | ((unsigned)outv[2 * e + 1] << 16);
    *(u32x4*)(yT + (size_t)(d0 + c2) * S_LEN + sbase) = o;
  }
}

__device__ __forceinline__ float wave_sum(float v) {
#pragma unroll
  for (int o = 32; o > 0; o >>= 1) v += __shfl_xor(v, o);
  return v;
}

__global__ __launch_bounds__(512) void k2_rowwise(const u16* __restrict__ q, const u16* __restrict__ yT,
                                                  const float* __restrict__ mod, u16* __restrict__ x1out,
                                                  u16* __restrict__ h2out) {
  __shared__ __attribute__((aligned(16))) u16 ys[8][D_DIM + 8];
  const int tid = threadIdx.x;
  const int lane = tid & 63;
  const int wid = tid >> 6;
  const int s0 = blockIdx.x * 8;
  if (tid < D_DIM) {
    u32x4 v = *(const u32x4*)(yT + (size_t)tid * S_LEN + s0);
#pragma unroll
    for (int e = 0; e < 4; ++e) {
      ys[2 * e][tid] = (u16)(v[e] & 0xffff);
      ys[2 * e + 1][tid] = (u16)(v[e] >> 16);
    }
  }
  __syncthreads();
  const int s = s0 + wid;
  const bool act = lane < 56;
  const int c0 = lane * 8;
  float x[8], yv[8];
  float sum = 0.f;
  if (act) {
    u32x4 qv = *(const u32x4*)(q + (size_t)s * D_DIM + c0);
    u32x4 yy = *(const u32x4*)(&ys[wid][c0]);
#pragma unroll
    for (int e = 0; e < 4; ++e) {
      x[2 * e] = lo16(qv[e]); x[2 * e + 1] = hi16(qv[e]);
      yv[2 * e] = lo16(yy[e]); yv[2 * e + 1] = hi16(yy[e]);
    }
#pragma unroll
    for (int e = 0; e < 8; ++e) sum += x[e];
  } else {
#pragma unroll
    for (int e = 0; e < 8; ++e) { x[e] = 0.f; yv[e] = 0.f; }
  }
  float mean = wave_sum(sum) * (1.0f / D_DIM);
  float sq = 0.f;
  if (act) {
#pragma unroll
    for (int e = 0; e < 8; ++e) { float dd = x[e] - mean; sq += dd * dd; }
  }
  float var = wave_sum(sq) * (1.0f / D_DIM);
  float m_b = rbf(mean);
  float sd = rbf(sqrtf(rbf(rbf(var) + 1e-5f)));
  float x1[8];
  float sum2 = 0.f;
  if (act) {
#pragma unroll
    for (int e = 0; e < 8; ++e) {
      int c = c0 + e;
      float sa = mod[0 * D_DIM + c], ca = mod[1 * D_DIM + c], ga = mod[2 * D_DIM + c];
      float n = rbf(rbf(x[e] - m_b) / sd);
      float h = rbf(rbf(n * rbf(1.0f + ca)) + sa);
      float a = rbf(h * yv[e]);
      x1[e] = rbf(x[e] + rbf(ga * a));
      sum2 += x1[e];
    }
  } else {
#pragma unroll
    for (int e = 0; e < 8; ++e) x1[e] = 0.f;
  }
  float mean2 = wave_sum(sum2) * (1.0f / D_DIM);
  float sq2 = 0.f;
  if (act) {
#pragma unroll
    for (int e = 0; e < 8; ++e) { float dd = x1[e] - mean2; sq2 += dd * dd; }
  }
  float var2 = wave_sum(sq2) * (1.0f / D_DIM);
  float m2_b = rbf(mean2);
  float sd2 = rbf(sqrtf(rbf(rbf(var2) + 1e-5f)));
  if (act) {
    u32x4 xo, ho;
    float hh[8];
#pragma unroll
    for (int e = 0; e < 8; ++e) {
      int c = c0 + e;
      float sb = mod[3 * D_DIM + c], cb = mod[4 * D_DIM + c];
      float n = rbf(rbf(x1[e] - m2_b) / sd2);
      hh[e] = rbf(rbf(n * rbf(1.0f + cb)) + sb);
    }
#pragma unroll
    for (int e = 0; e < 4; ++e) {
      xo[e] = pack2(x1[2 * e], x1[2 * e + 1]);
      ho[e] = pack2(hh[2 * e], hh[2 * e + 1]);
    }
    *(u32x4*)(x1out + (size_t)s * D_DIM + c0) = xo;
    *(u32x4*)(h2out + (size_t)s * D_DIM + c0) = ho;
  }
}

struct GemmArgs {
  const u16* A;
  const u16* B;
  int lda, ldb;
  int K, pad;
  u16* out;
  const u16* z;
  float2* stats;
  const u16* bsp;
  const u16* x1;
  const float* mod;
};

__device__ __forceinline__ int a_swz(int row) { return row & 7; }
template <int BN>
__device__ __forceinline__ int b_swz(int row) {
  if (BN == 128) return ((row & 3) << 2) | ((row >> 2) & 3);
  else return (((row >> 1) & 1) << 1) | (((row >> 3) & 1) << 2);
}
__device__ __forceinline__ unsigned a_off(int row, int ch) { return (unsigned)(row * 128 + 16 * (ch ^ a_swz(row))); }
template <int BN>
__device__ __forceinline__ unsigned b_off(int row, int ch) { return (unsigned)(BN * 2 * row + 16 * (ch ^ b_swz<BN>(row))); }

__device__ __forceinline__ float gelu_tanh(float x) {
  const float k0 = 0.7978845608028654f, k1 = 0.044715f;
  float u = k0 * (x + k1 * x * x * x);
  float t = 1.0f - 2.0f / (1.0f + __expf(2.0f * u));
  return 0.5f * x * (1.0f + t);
}

template <int BM, int BN, int MODE, int NST>
__global__ __launch_bounds__(512) void gemm_kernel(GemmArgs g) {
  constexpr int BK = 64;
  constexpr int NW = 8;
  constexpr int WM = BM / 4, WN = BN / 2;
  constexpr int MI = WM / 16, NI = WN / 16;
  constexpr int A_BYTES = BM * BK * 2;
  constexpr int B_BYTES = BK * BN * 2;
  constexpr int ST_BYTES = A_BYTES + B_BYTES;
  constexpr int A_INS = A_BYTES / (1024 * NW);
  constexpr int B_INS = B_BYTES / (1024 * NW);
  constexpr int INS = A_INS + B_INS;
  constexpr int CPR = BN / 8;
  extern __shared__ __attribute__((aligned(16))) char smem[];

  const int tid = threadIdx.x;
  const int lane = tid & 63;
  const int wid = __builtin_amdgcn_readfirstlane(tid >> 6);
  const int wm0 = (wid >> 1) * WM;
  const int wn0 = (wid & 1) * WN;
  const int m0 = blockIdx.y * BM;
  const int n0 = blockIdx.x * BN;
  const int nk = g.K / BK;

  f32x4 acc[MI][NI];
#pragma unroll
  for (int i = 0; i < MI; ++i)
#pragma unroll
    for (int jn = 0; jn < NI; ++jn) acc[i][jn] = (f32x4){0.f, 0.f, 0.f, 0.f};

  const u16* a_src[A_INS];
  const u16* b_src[B_INS];
#pragma unroll
  for (int i = 0; i < A_INS; ++i) {
    int L = (wid * A_INS + i) * 64 + lane;
    int row = L >> 3, chp = L & 7;
    int ch = chp ^ a_swz(row);
    a_src[i] = g.A + (size_t)(m0 + row) * g.lda + ch * 8;
  }
#pragma unroll
  for (int i = 0; i < B_INS; ++i) {
    int L = (wid * B_INS + i) * 64 + lane;
    int row = L / CPR, chp = L % CPR;
    int ch = chp ^ b_swz<BN>(row);
    b_src[i] = g.B + (size_t)row * g.ldb + n0 + ch * 8;
  }
  auto issue = [&](int kt) {
    const int st = kt % NST;
    char* As = smem + st * ST_BYTES;
    char* Bs = As + A_BYTES;
#pragma unroll
    for (int i = 0; i < A_INS; ++i)
      __builtin_amdgcn_global_load_lds((const void*)(a_src[i] + kt * BK),
                                       (__attribute__((address_space(3))) void*)(As + (wid * A_INS + i) * 1024), 16, 0, 0);
#pragma unroll
    for (int i = 0; i < B_INS; ++i)
      __builtin_amdgcn_global_load_lds((const void*)(b_src[i] + (size_t)kt * BK * g.ldb),
                                       (__attribute__((address_space(3))) void*)(Bs + (wid * B_INS + i) * 1024), 16, 0, 0);
  };
  auto wait_stage = [&](int target, int last) {
    const int rem = last - target;
    if (NST > 7 && rem >= 7) asm volatile("s_waitcnt vmcnt(%0)" ::"n"(7 * INS) : "memory");
    else if (NST > 6 && rem == 6) asm volatile("s_waitcnt vmcnt(%0)" ::"n"(6 * INS) : "memory");
    else if (NST > 5 && rem == 5) asm volatile("s_waitcnt vmcnt(%0)" ::"n"(5 * INS) : "memory");
    else if (NST > 4 && rem == 4) asm volatile("s_waitcnt vmcnt(%0)" ::"n"(4 * INS) : "memory");
    else if (NST > 3 && rem == 3) asm volatile("s_waitcnt vmcnt(%0)" ::"n"(3 * INS) : "memory");
    else if (rem == 2) asm volatile("s_waitcnt vmcnt(%0)" ::"n"(2 * INS) : "memory");
    else if (rem == 1) asm volatile("s_waitcnt vmcnt(%0)" ::"n"(INS) : "memory");
    else asm volatile("s_waitcnt vmcnt(0)" ::: "memory");
  };

  u32x4 fa[2][MI];
  u32x2 fb[2][NI][2];
  const unsigned sbase = (unsigned)(size_t)smem;
  auto read_frags = [&](int kt, int ks, int slot) {
    const int st = kt % NST;
    const unsigned ab = sbase + st * ST_BYTES;
    const unsigned bb = ab + A_BYTES;
#pragma unroll
    for (int i = 0; i < MI; ++i) {
      int row = wm0 + i * 16 + (lane & 15);
      unsigned o = ab + a_off(row, ks * 4 + (lane >> 4));
      asm volatile("ds_read_b128 %0, %1" : "=v"(fa[slot][i]) : "v"(o));
    }
#pragma unroll
    for (int jn = 0; jn < NI; ++jn) {
      const int gq = lane >> 4, qq = (lane & 15) >> 2, pp = lane & 3;
      const int c = (wn0 + jn * 16) >> 4;
      unsigned o0 = bb + b_off<BN>(32 * ks + 8 * gq + 0 + qq, 2 * c + (pp >> 1)) + 8 * (pp & 1);
      unsigned o1 = bb + b_off<BN>(32 * ks + 8 * gq + 4 + qq, 2 * c + (pp >> 1)) + 8 * (pp & 1);
      asm volatile("ds_read_b64_tr_b16 %0, %1" : "=v"(fb[slot][jn][0]) : "v"(o0));
      asm volatile("ds_read_b64_tr_b16 %0, %1" : "=v"(fb[slot][jn][1]) : "v"(o1));
    }
  };
  auto do_mfma = [&](int slot) {
#pragma unroll
    for (int jn = 0; jn < NI; ++jn) {
      u32x4 bu = {fb[slot][jn][0][0], fb[slot][jn][0][1], fb[slot][jn][1][0], fb[slot][jn][1][1]};
      s16x8 bfr = __builtin_bit_cast(s16x8, bu);
#pragma unroll
      for (int i = 0; i < MI; ++i)
        acc[i][jn] = __builtin_amdgcn_mfma_f32_16x16x32_bf16(__builtin_bit_cast(s16x8, fa[slot][i]), bfr, acc[i][jn], 0, 0, 0);
    }
  };

  int last = -1;
#pragma unroll
  for (int i = 0; i < NST - 1; ++i)
    if (i < nk) { issue(i); last = i; }
  wait_stage(0, last);
  __builtin_amdgcn_s_barrier();
  read_frags(0, 0, 0);
  for (int kt = 0; kt < nk; ++kt) {
    if (kt + NST - 1 < nk) { issue(kt + NST - 1); last = kt + NST - 1; }
    asm volatile("s_waitcnt lgkmcnt(0)" ::: "memory");
    __builtin_amdgcn_sched_barrier(0);
    read_frags(kt, 1, 1);
    do_mfma(0);
    asm volatile("s_waitcnt lgkmcnt(0)" ::: "memory");
    __builtin_amdgcn_sched_barrier(0);
    if (kt + 1 < nk) {
      wait_stage(kt + 1, last);
      __builtin_amdgcn_s_barrier();
      read_frags(kt + 1, 0, 0);
    }
    do_mfma(1);
  }
  asm volatile("s_waitcnt lgkmcnt(0)" ::: "memory");
  __builtin_amdgcn_s_barrier();

  constexpr int LDC = BN + 4;
  float* Cs = (float*)smem;
#pragma unroll
  for (int i = 0; i < MI; ++i)
#pragma unroll
    for (int jn = 0; jn < NI; ++jn)
#pragma unroll
      for (int r = 0; r < 4; ++r) {
        int row = wm0 + i * 16 + (lane >> 4) * 4 + r;
        int col = wn0 + jn * 16 + (lane & 15);
        Cs[row * LDC + col] = acc[i][jn][r];
      }
  __syncthreads();
  constexpr int CH_PER_ROW = BN / 8;
  constexpr int ITER = BM * CH_PER_ROW / 512;
#pragma unroll
  for (int it = 0; it < ITER; ++it) {
    int c = tid + 512 * it;
    int row = c / CH_PER_ROW, ch = c % CH_PER_ROW;
    int grow = m0 + row, gcol = n0 + ch * 8;
    float v[8];
    f32x4 c0 = *(const f32x4*)(&Cs[row * LDC + ch * 8]);
    f32x4 c1 = *(const f32x4*)(&Cs[row * LDC + ch * 8 + 4]);
#pragma unroll
    for (int e = 0; e < 4; ++e) { v[e] = rbf(c0[e]); v[4 + e] = rbf(c1[e]); }
    if (MODE == 1) {
#pragma unroll
      for (int e = 0; e < 8; ++e) v[e] = rbf(gelu_tanh(v[e]));
      u32x4 o;
#pragma unroll
      for (int e = 0; e < 4; ++e) o[e] = pack2(v[2 * e], v[2 * e + 1]);
      *(u32x4*)(g.out + (size_t)grow * F_DIM + gcol) = o;
      if (n0 >= FH_DIM) {
        float sm = 0.f;
#pragma unroll
        for (int e = 0; e < 8; ++e) sm += v[e];
#pragma unroll
        for (int o2 = 8; o2 > 0; o2 >>= 1) sm += __shfl_xor(sm, o2);
        float mean = sm * (1.0f / 128.0f);
        float m2 = 0.f;
#pragma unroll
        for (int e = 0; e < 8; ++e) { float dd = v[e] - mean; m2 += dd * dd; }
#pragma unroll
        for (int o2 = 8; o2 > 0; o2 >>= 1) m2 += __shfl_xor(m2, o2);
        if (ch == 0) g.stats[(size_t)grow * 7 + (n0 - FH_DIM) / 128] = make_float2(mean, m2);
      }
    } else if (MODE == 2) {
      float b = bf2f(g.bsp[grow]);
      u32x4 z1 = *(const u32x4*)(g.z + (size_t)grow * F_DIM + gcol);
      u32x4 o;
#pragma unroll
      for (int e = 0; e < 4; ++e) {
        float g0 = rbf(v[2 * e] + b), g1 = rbf(v[2 * e + 1] + b);
        o[e] = pack2(lo16(z1[e]) * g0, hi16(z1[e]) * g1);
      }
      *(u32x4*)(g.out + (size_t)grow * FH_DIM + gcol) = o;
    } else {
      u32x4 xv = *(const u32x4*)(g.x1 + (size_t)grow * D_DIM + gcol);
      u32x4 o;
#pragma unroll
      for (int e = 0; e < 4; ++e) {
        float gb0 = g.mod[5 * D_DIM + gcol + 2 * e], gb1 = g.mod[5 * D_DIM + gcol + 2 * e + 1];
        o[e] = pack2(lo16(xv[e]) + rbf(gb0 * v[2 * e]), hi16(xv[e]) + rbf(gb1 * v[2 * e + 1]));
      }
      *(u32x4*)(g.out + (size_t)grow * D_DIM + gcol) = o;
    }
  }
}

__global__ __launch_bounds__(256) void k3b_ln_z2(const u16* __restrict__ z, const float2* __restrict__ stats,
                                                 u16* __restrict__ z2n) {
  const int row = blockIdx.x * 4 + (threadIdx.x >> 6);
  const int lane = threadIdx.x & 63;
  const float2* st = stats + (size_t)row * 7;
  float mean = 0.f;
#pragma unroll
  for (int b = 0; b < 7; ++b) mean += st[b].x;
  mean *= (1.0f / 7.0f);
  float m2 = 0.f;
#pragma unroll
  for (int b = 0; b < 7; ++b) { float dd = st[b].x - mean; m2 += st[b].y + 128.0f * dd * dd; }
  float var = m2 * (1.0f / FH_DIM);
  float mb = rbf(mean);
  float sd = rbf(sqrtf(rbf(rbf(var) + 1e-5f)));
#pragma unroll
  for (int i = 0; i < 2; ++i) {
    int c = lane + 64 * i;
    if (c < 112) {
      u32x4 v = *(const u32x4*)(z + (size_t)row * F_DIM + FH_DIM + c * 8);
#pragma unroll
      for (int e = 0; e < 4; ++e) {
        float a = rbf(rbf(lo16(v[e]) - mb) / sd);
        float b2 = rbf(rbf(hi16(v[e]) - mb) / sd);
        v[e] = pack2(a, b2);
      }
      *(u32x4*)(z2n + (size_t)row * FH_DIM + c * 8) = v;
    }
  }
}

extern "C" void kernel_launch(void* const* d_in, const int* in_sizes, int n_in, void* d_out, int out_size,
                              void* d_ws, size_t ws_size, hipStream_t stream) {
  const u16* q = (const u16*)d_in[0];
  const u16* k = (const u16*)d_in[1];
  const u16* v = (const u16*)d_in[2];
  const u16* shiftk = (const u16*)d_in[3];
  const u16* ssmk = (const u16*)d_in[4];
  const u16* ssmD = (const u16*)d_in[5];
  const u16* w_in = (const u16*)d_in[7];
  const u16* w_sp = (const u16*)d_in[8];
  const u16* b_sp = (const u16*)d_in[9];
  const u16* w_out = (const u16*)d_in[10];
  const u16* cvec = (const u16*)d_in[11];
  const u16* w_mod = (const u16*)d_in[12];
  u16* out = (u16*)d_out;

  char* ws = (char*)d_ws;
  size_t off = 0;
  float* mod = (float*)(ws + off); off += 16384;
  u16* yT = (u16*)(ws + off); off += (size_t)D_DIM * S_LEN * 2;
  u16* x1 = (u16*)(ws + off); off += (size_t)S_LEN * D_DIM * 2;
  u16* h2 = (u16*)(ws + off); off += (size_t)S_LEN * D_DIM * 2;
  u16* z = (u16*)(ws + off); off += (size_t)S_LEN * F_DIM * 2;
  float2* stats = (float2*)(ws + off); off += (size_t)S_LEN * 7 * 8;
  u16* t = (u16*)(ws + off); off += (size_t)S_LEN * FH_DIM * 2;
  u16* z2n = (u16*)(ws + off); off += (size_t)S_LEN * FH_DIM * 2;

  hipFuncSetAttribute((const void*)k1_conv_mod, hipFuncAttributeMaxDynamicSharedMemorySize, 2 * CH_BYTES);
  hipFuncSetAttribute((const void*)gemm_kernel<128, 128, 1, 4>, hipFuncAttributeMaxDynamicSharedMemorySize, 131072);
  hipFuncSetAttribute((const void*)gemm_kernel<128, 64, 2, 6>, hipFuncAttributeMaxDynamicSharedMemorySize, 147456);
  hipFuncSetAttribute((const void*)gemm_kernel<64, 64, 3, 8>, hipFuncAttributeMaxDynamicSharedMemorySize, 131072);
  hipLaunchKernelGGL(k1_conv_mod, dim3(K1_CONV_BLOCKS + K1_MOD_BLOCKS), dim3(512), 2 * CH_BYTES, stream, k, v,
                     shiftk, ssmk, ssmD, cvec, w_mod, mod, yT);
  hipLaunchKernelGGL(k2_rowwise, dim3(S_LEN / 8), dim3(512), 0, stream, q, yT, mod, x1, h2);

  GemmArgs g1;
  g1.A = h2; g1.B = w_in; g1.lda = D_DIM; g1.ldb = F_DIM; g1.K = D_DIM; g1.pad = 0;
  g1.out = z; g1.z = z; g1.stats = stats; g1.bsp = b_sp; g1.x1 = x1; g1.mod = mod;
  hipLaunchKernelGGL((gemm_kernel<128, 128, 1, 4>), dim3(F_DIM / 128, S_LEN / 128), dim3(512),
                     4 * (128 * 64 * 2 + 64 * 128 * 2), stream, g1);

  hipLaunchKernelGGL(k3b_ln_z2, dim3(S_LEN / 4), dim3(256), 0, stream, z, stats, z2n);

  GemmArgs g2;
  g2.A = w_sp; g2.B = z2n; g2.lda = S_LEN; g2.ldb = FH_DIM; g2.K = S_LEN; g2.pad = 0;
  g2.out = t; g2.z = z; g2.stats = stats; g2.bsp = b_sp; g2.x1 = x1; g2.mod = mod;
  hipLaunchKernelGGL((gemm_kernel<128, 64, 2, 6>), dim3(FH_DIM / 64, S_LEN / 128), dim3(512),
                     6 * (128 * 64 * 2 + 64 * 64 * 2), stream, g2);

  GemmArgs g3;
  g3.A = t; g3.B = w_out; g3.lda = FH_DIM; g3.ldb = D_DIM; g3.K = FH_DIM; g3.pad = 0;
  g3.out = out; g3.z = z; g3.stats = stats; g3.bsp = b_sp; g3.x1 = x1; g3.mod = mod;
  hipLaunchKernelGGL((gemm_kernel<64, 64, 3, 8>), dim3(D_DIM / 64, S_LEN / 64), dim3(512),
                     8 * (64 * 64 * 2 + 64 * 64 * 2), stream, g3);
}
```

```cpp
#include <hip/hip_runtime.h>
#include <hip/hip_bf16.h>
#include <stdint.h>

#define S_LEN 2048
#define D_DIM 448
#define F_DIM 1792
#define FH_DIM 896
#define MOD_N 2688

typedef unsigned short u16;
typedef __attribute__((ext_vector_type(8))) short s16x8;
typedef __attribute__((ext_vector_type(4))) short s16x4;
typedef __attribute__((ext_vector_type(4))) float f32x4;
typedef __attribute__((ext_vector_type(16))) float f32x16;
typedef __attribute__((ext_vector_type(4))) unsigned u32x4;
typedef __attribute__((ext_vector_type(2))) unsigned u32x2;

__device__ __forceinline__ float bf2f(u16 x) { return __uint_as_float(((unsigned)x) << 16); }
__device__ __forceinline__ u16 f2bf(float f) {
  __hip_bfloat16 b = __float2bfloat16(f);
  return *reinterpret_cast<u16*>(&b);
}
__device__ __forceinline__ float rbf(float f) { return bf2f(f2bf(f)); }
__device__ __forceinline__ float lo16(unsigned u) { return __uint_as_float(u << 16); }
__device__ __forceinline__ float hi16(unsigned u) { return __uint_as_float(u & 0xffff0000u); }
__device__ __forceinline__ unsigned pack2(float a, float b) {
  return (unsigned)f2bf(a) | ((unsigned)f2bf(b) << 16);
}

#define K1_CONV_BLOCKS 224
#define K1_MOD_BLOCKS 21
#define U_BLK_STRIDE 80
#define U_BYTES (96 * U_BLK_STRIDE)
#define KR_STRIDE 4160
#define CH_BYTES 40960
#define KR_OFF 7680
#define RED_OFF 8192

__global__ __launch_bounds__(512, 1) void k1_conv_mod(
    const u16* __restrict__ kin, const u16* __restrict__ vin, const u16* __restrict__ shiftk,
    const u16* __restrict__ ssmk, const u16* __restrict__ ssmD, const u16* __restrict__ cvec,
    const u16* __restrict__ wmod, float* __restrict__ mod_out, u16* __restrict__ yT) {
  extern __shared__ __attribute__((aligned(16))) char smem[];
  const int tid = threadIdx.x;
  const int lane = tid & 63;
  const int wid = tid >> 6;

  if (blockIdx.x >= K1_CONV_BLOCKS) {
    const int cb = blockIdx.x - K1_CONV_BLOCKS;
    float* red = (float*)smem;
    float* sc = (float*)(smem + 8 * 128 * 4);
    if (tid < D_DIM) {
      float x = bf2f(cvec[tid]);
      sc[tid] = rbf(x / (1.0f + __expf(-x)));
    }
    __syncthreads();
    const int col = cb * 128 + lane * 2;
    float a0 = 0.f, a1 = 0.f;
    const int r0 = wid * 56;
#pragma unroll 8
    for (int r = 0; r < 56; ++r) {
      unsigned w = *(const unsigned*)(wmod + (size_t)(r0 + r) * MOD_N + col);
      float s = sc[r0 + r];
      a0 += s * lo16(w);
      a1 += s * hi16(w);
    }
    red[wid * 128 + lane * 2] = a0;
    red[wid * 128 + lane * 2 + 1] = a1;
    __syncthreads();
    if (tid < 128) {
      float s = 0.f;
#pragma unroll
      for (int w = 0; w < 8; ++w) s += red[w * 128 + tid];
      mod_out[cb * 128 + tid] = rbf(s);
    }
    return;
  }

  const int d0 = blockIdx.x * 2;
  for (int i = tid; i < 2 * 32 * U_BLK_STRIDE / 4; i += 512) {
    int ch = i / (32 * U_BLK_STRIDE / 4);
    int o = i % (32 * U_BLK_STRIDE / 4);
    *(unsigned*)(smem + ch * CH_BYTES + o * 4) = 0u;
  }
  {
    const int ch = tid >> 8;
    const int t = tid & 255;
    u32x4 kk = *(const u32x4*)(ssmk + (size_t)(d0 + ch) * S_LEN + t * 8);
    char* kr = smem + ch * CH_BYTES + KR_OFF;
    u16 taps[8];
#pragma unroll
    for (int e = 0; e < 4; ++e) { taps[2 * e] = (u16)(kk[e] & 0xffff); taps[2 * e + 1] = (u16)(kk[e] >> 16); }
#pragma unroll
    for (int e = 0; e < 8; ++e) {
      int p = 2047 - (t * 8 + e);
#pragma unroll
      for (int s = 0; s < 4; ++s) {
        int q = p - s;
        if (q >= 0) *(u16*)(kr + s * KR_STRIDE + q * 2) = taps[e];
      }
    }
    if (tid < 2 * 4 * 32) {
      int c2 = tid >> 7, s = (tid >> 5) & 3, e = tid & 31;
      int q = 2048 + e - s;
      *(u16*)(smem + c2 * CH_BYTES + KR_OFF + s * KR_STRIDE + q * 2) = 0;
    }
  }
  {
    const int s0 = tid * 4;
    unsigned kr[4], vr[4], kh[3];
#pragma unroll
    for (int i = 0; i < 4; ++i) {
      kr[i] = *(const unsigned*)(kin + (size_t)(s0 + i) * D_DIM + d0);
      vr[i] = *(const unsigned*)(vin + (size_t)(s0 + i) * D_DIM + d0);
    }
#pragma unroll
    for (int i = 0; i < 3; ++i) {
      unsigned fromprev = __shfl_up(kr[i + 1], 1);
      unsigned g = 0u;
      if (lane == 0) {
        int s = s0 - 3 + i;
        if (s >= 0) g = *(const unsigned*)(kin + (size_t)s * D_DIM + d0);
      }
      kh[i] = (lane == 0) ? g : fromprev;
    }
    float sk0[4], sk1[4];
#pragma unroll
    for (int j = 0; j < 4; ++j) {
      sk0[j] = bf2f(shiftk[(d0)*4 + j]);
      sk1[j] = bf2f(shiftk[(d0 + 1) * 4 + j]);
    }
    unsigned kall[7] = {kh[0], kh[1], kh[2], kr[0], kr[1], kr[2], kr[3]};
#pragma unroll
    for (int i = 0; i < 4; ++i) {
      float a0 = 0.f, a1 = 0.f;
#pragma unroll
      for (int t = 0; t < 4; ++t) {
        a0 = rbf(a0 + rbf(lo16(kall[i + t]) * sk0[3 - t]));
        a1 = rbf(a1 + rbf(hi16(kall[i + t]) * sk1[3 - t]));
      }
      float kv0 = rbf(a0 * lo16(vr[i]));
      float kv1 = rbf(a1 * hi16(vr[i]));
      int s = s0 + i;
      int off = (32 + (s >> 5)) * U_BLK_STRIDE + (s & 31) * 2;
      *(u16*)(smem + off) = f2bf(kv0);
      *(u16*)(smem + CH_BYTES + off) = f2bf(kv1);
    }
  }
  __syncthreads();

  const int ch = wid >> 2;
  const int wq = wid & 3;
  const char* U = smem + ch * CH_BYTES;
  const char* KR = smem + ch * CH_BYTES + KR_OFF;
  const int j = lane & 31;
  const int kg = lane >> 5;
  f32x16 acc0, acc1;
#pragma unroll
  for (int i = 0; i < 16; ++i) { acc0[i] = 0.f; acc1[i] = 0.f; }
  for (int dl = wq; dl < 64; dl += 4) {
#pragma unroll
    for (int h = 0; h < 2; ++h) {
      int n = 32 * dl + j - 16 * h - 8 * kg;
      int p0 = 2047 - n;
      int s = p0 & 3;
      int q = p0 - s;
      const char* ap = KR + s * KR_STRIDE + q * 2;
      u32x2 a_lo = *(const u32x2*)(ap);
      u32x2 a_hi = *(const u32x2*)(ap + 8);
      u32x4 au = {a_lo[0], a_lo[1], a_hi[0], a_hi[1]};
      s16x8 afrag = __builtin_bit_cast(s16x8, au);
      {
        int blk = 32 + (32 + j - dl);
        u32x4 bu = *(const u32x4*)(U + blk * U_BLK_STRIDE + (16 * h + 8 * kg) * 2);
        acc1 = __builtin_amdgcn_mfma_f32_32x32x16_bf16(afrag, __builtin_bit_cast(s16x8, bu), acc1, 0, 0, 0);
      }
      if (dl < 32) {
        int blk = 32 + (j - dl);
        u32x4 bu = *(const u32x4*)(U + blk * U_BLK_STRIDE + (16 * h + 8 * kg) * 2);
        acc0 = __builtin_amdgcn_mfma_f32_32x32x16_bf16(afrag, __builtin_bit_cast(s16x8, bu), acc0, 0, 0, 0);
      }
    }
  }
  __syncthreads();
  {
    float* red = (float*)(smem + ch * CH_BYTES + RED_OFF);
#pragma unroll
    for (int r = 0; r < 16; ++r) {
      int row = (r & 3) + 8 * (r >> 2) + 4 * kg;
      red[((wq * 2 + 0) * 32 + row) * 32 + j] = acc0[r];
      red[((wq * 2 + 1) * 32 + row) * 32 + j] = acc1[r];
    }
  }
  __syncthreads();
  {
    const int c2 = tid >> 8;
    const int t = tid & 255;
    const int sbase = t * 8;
    const float* red = (const float*)(smem + c2 * CH_BYTES + RED_OFF);
    const char* Uc = smem + c2 * CH_BYTES;
    const float Dv = bf2f(ssmD[d0 + c2]);
    u16 outv[8];
#pragma unroll
    for (int e = 0; e < 8; ++e) {
      int s = sbase + e;
      int i = s >> 5, jj = s & 31;
      int tile = i >> 5, col = i & 31;
      float sum = 0.f;
#pragma unroll
      for (int w = 0; w < 4; ++w) sum += red[((w * 2 + tile) * 32 + jj) * 32 + col];
      float y = rbf(sum);
      float kv = bf2f(*(const u16*)(Uc + (32 + i) * U_BLK_STRIDE + jj * 2));
      y = rbf(y + rbf(kv * Dv));
      outv[e] = f2bf(y);
    }
    u32x4 o;
#pragma unroll
    for (int e = 0; e < 4; ++e) o[e] = (unsigned)outv[2 * e] | ((unsigned)outv[2 * e + 1] << 16);
    *(u32x4*)(yT + (size_t)(d0 + c2) * S_LEN + sbase) = o;
  }
}

__device__ __forceinline__ float wave_sum(float v) {
#pragma unroll
  for (int o = 32; o > 0; o >>= 1) v += __shfl_xor(v, o);
  return v;
}

__global__ __launch_bounds__(512) void k2_rowwise(const u16* __restrict__ q, const u16* __restrict__ yT,
                                                  const float* __restrict__ mod, u16* __restrict__ x1out,
                                                  u16* __restrict__ h2out) {
  __shared__ __attribute__((aligned(16))) u16 ys[8][D_DIM + 8];
  const int tid = threadIdx.x;
  const int lane = tid & 63;
  const int wid = tid >> 6;
  const int s0 = blockIdx.x * 8;
  if (tid < D_DIM) {
    u32x4 v = *(const u32x4*)(yT + (size_t)tid * S_LEN + s0);
#pragma unroll
    for (int e = 0; e < 4; ++e) {
      ys[2 * e][tid] = (u16)(v[e] & 0xffff);
      ys[2 * e + 1][tid] = (u16)(v[e] >> 16);
    }
  }
  __syncthreads();
  const int s = s0 + wid;
  const bool act = lane < 56;
  const int c0 = lane * 8;
  float x[8], yv[8];
  float msa[8], mca[8], mga[8], msb[8], mcb[8];
  {
    const int cc = act ? c0 : 0;
#pragma unroll
    for (int hh = 0; hh < 2; ++hh) {
      f32x4 a0 = *(const f32x4*)(mod + 0 * D_DIM + cc + 4 * hh);
      f32x4 a1 = *(const f32x4*)(mod + 1 * D_DIM + cc + 4 * hh);
      f32x4 a2 = *(const f32x4*)(mod + 2 * D_DIM + cc + 4 * hh);
      f32x4 a3 = *(const f32x4*)(mod + 3 * D_DIM + cc + 4 * hh);
      f32x4 a4 = *(const f32x4*)(mod + 4 * D_DIM + cc + 4 * hh);
#pragma unroll
      for (int e = 0; e < 4; ++e) {
        msa[4 * hh + e] = a0[e]; mca[4 * hh + e] = a1[e]; mga[4 * hh + e] = a2[e];
        msb[4 * hh + e] = a3[e]; mcb[4 * hh + e] = a4[e];
      }
    }
  }
  float sum = 0.f;
  if (act) {
    u32x4 qv = *(const u32x4*)(q + (size_t)s * D_DIM + c0);
    u32x4 yy = *(const u32x4*)(&ys[wid][c0]);
#pragma unroll
    for (int e = 0; e < 4; ++e) {
      x[2 * e] = lo16(qv[e]); x[2 * e + 1] = hi16(qv[e]);
      yv[2 * e] = lo16(yy[e]); yv[2 * e + 1] = hi16(yy[e]);
    }
#pragma unroll
    for (int e = 0; e < 8; ++e) sum += x[e];
  } else {
#pragma unroll
    for (int e = 0; e < 8; ++e) { x[e] = 0.f; yv[e] = 0.f; }
  }
  float mean = wave_sum(sum) * (1.0f / D_DIM);
  float sq = 0.f;
  if (act) {
#pragma unroll
    for (int e = 0; e < 8; ++e) { float dd = x[e] - mean; sq += dd * dd; }
  }
  float var = wave_sum(sq) * (1.0f / D_DIM);
  float m_b = rbf(mean);
  float sd = rbf(sqrtf(rbf(rbf(var) + 1e-5f)));
  float x1[8];
  float sum2 = 0.f;
  if (act) {
#pragma unroll
    for (int e = 0; e < 8; ++e) {
      int c = c0 + e;
      float sa = msa[e], ca = mca[e], ga = mga[e]; (void)c;
      float n = rbf(rbf(x[e] - m_b) / sd);
      float h = rbf(rbf(n * rbf(1.0f + ca)) + sa);
      float a = rbf(h * yv[e]);
      x1[e] = rbf(x[e] + rbf(ga * a));
      sum2 += x1[e];
    }
  } else {
#pragma unroll
    for (int e = 0; e < 8; ++e) x1[e] = 0.f;
  }
  float mean2 = wave_sum(sum2) * (1.0f / D_DIM);
  float sq2 = 0.f;
  if (act) {
#pragma unroll
    for (int e = 0; e < 8; ++e) { float dd = x1[e] - mean2; sq2 += dd * dd; }
  }
  float var2 = wave_sum(sq2) * (1.0f / D_DIM);
  float m2_b = rbf(mean2);
  float sd2 = rbf(sqrtf(rbf(rbf(var2) + 1e-5f)));
  if (act) {
    u32x4 xo, ho;
    float hh[8];
#pragma unroll
    for (int e = 0; e < 8; ++e) {
      int c = c0 + e;
      float sb = msb[e], cb = mcb[e]; (void)c;
      float n = rbf(rbf(x1[e] - m2_b) / sd2);
      hh[e] = rbf(rbf(n * rbf(1.0f + cb)) + sb);
    }
#pragma unroll
    for (int e = 0; e < 4; ++e) {
      xo[e] = pack2(x1[2 * e], x1[2 * e + 1]);
      ho[e] = pack2(hh[2 * e], hh[2 * e + 1]);
    }
    *(u32x4*)(x1out + (size_t)s * D_DIM + c0) = xo;
    *(u32x4*)(h2out + (size_t)s * D_DIM + c0) = ho;
  }
}

struct GemmArgs {
  const u16* A;
  const u16* B;
  int lda, ldb;
  int K, pad;
  u16* out;
  const u16* z;
  float2* stats;
  const u16* bsp;
  const u16* x1;
  const float* mod;
};

__device__ __forceinline__ int a_swz(int row) { return row & 7; }
template <int BN>
__device__ __forceinline__ int b_swz(int row) {
  if (BN == 128) return ((row & 3) << 2) | ((row >> 2) & 3);
  else return (((row >> 1) & 1) << 1) | (((row >> 3) & 1) << 2);
}
__device__ __forceinline__ unsigned a_off(int row, int ch) { return (unsigned)(row * 128 + 16 * (ch ^ a_swz(row))); }
template <int BN>
__device__ __forceinline__ unsigned b_off(int row, int ch) { return (unsigned)(BN * 2 * row + 16 * (ch ^ b_swz<BN>(row))); }

__device__ __forceinline__ float gelu_tanh(float x) {
  const float k0 = 0.7978845608028654f, k1 = 0.044715f;
  float u = k0 * (x + k1 * x * x * x);
  float t = 1.0f - 2.0f / (1.0f + __expf(2.0f * u));
  return 0.5f * x * (1.0f + t);
}

template <int BM, int BN, int MODE, int NST>
__global__ __launch_bounds__(512) void gemm_kernel(GemmArgs g) {
  constexpr int BK = 64;
  constexpr int NW = 8;
  constexpr int WM = BM / 4, WN = BN / 2;
  constexpr int MI = WM / 16, NI = WN / 16;
  constexpr int A_BYTES = BM * BK * 2;
  constexpr int B_BYTES = BK * BN * 2;
  constexpr int ST_BYTES = A_BYTES + B_BYTES;
  constexpr int A_INS = A_BYTES / (1024 * NW);
  constexpr int B_INS = B_BYTES / (1024 * NW);
  constexpr int INS = A_INS + B_INS;
  constexpr int CPR = BN / 8;
  extern __shared__ __attribute__((aligned(16))) char smem[];

  const int tid = threadIdx.x;
  const int lane = tid & 63;
  const int wid = __builtin_amdgcn_readfirstlane(tid >> 6);
  const int wm0 = (wid >> 1) * WM;
  const int wn0 = (wid & 1) * WN;
  const int nwg = gridDim.x * gridDim.y;
  const int lin = blockIdx.x + gridDim.x * blockIdx.y;
  const int rem_id = (lin & 7) * (nwg >> 3) + (lin >> 3);
  const int m0 = (rem_id / gridDim.x) * BM;
  const int n0 = (rem_id % gridDim.x) * BN;
  const int nk = g.K / BK;

  f32x4 acc[MI][NI];
#pragma unroll
  for (int i = 0; i < MI; ++i)
#pragma unroll
    for (int jn = 0; jn < NI; ++jn) acc[i][jn] = (f32x4){0.f, 0.f, 0.f, 0.f};

  const u16* a_src[A_INS];
  const u16* b_src[B_INS];
#pragma unroll
  for (int i = 0; i < A_INS; ++i) {
    int L = (wid * A_INS + i) * 64 + lane;
    int row = L >> 3, chp = L & 7;
    int ch = chp ^ a_swz(row);
    a_src[i] = g.A + (size_t)(m0 + row) * g.lda + ch * 8;
  }
#pragma unroll
  for (int i = 0; i < B_INS; ++i) {
    int L = (wid * B_INS + i) * 64 + lane;
    int row = L / CPR, chp = L % CPR;
    int ch = chp ^ b_swz<BN>(row);
    b_src[i] = g.B + (size_t)row * g.ldb + n0 + ch * 8;
  }
  auto issue = [&](int kt) {
    const int st = kt % NST;
    char* As = smem + st * ST_BYTES;
    char* Bs = As + A_BYTES;
#pragma unroll
    for (int i = 0; i < A_INS; ++i)
      __builtin_amdgcn_global_load_lds((const void*)(a_src[i] + kt * BK),
                                       (__attribute__((address_space(3))) void*)(As + (wid * A_INS + i) * 1024), 16, 0, 0);
#pragma unroll
    for (int i = 0; i < B_INS; ++i)
      __builtin_amdgcn_global_load_lds((const void*)(b_src[i] + (size_t)kt * BK * g.ldb),
                                       (__attribute__((address_space(3))) void*)(Bs + (wid * B_INS + i) * 1024), 16, 0, 0);
  };
  auto wait_stage = [&](int target, int last) {
    const int rem = last - target;
    if (NST > 7 && rem >= 7) asm volatile("s_waitcnt vmcnt(%0)" ::"n"(7 * INS) : "memory");
    else if (NST > 6 && rem == 6) asm volatile("s_waitcnt vmcnt(%0)" ::"n"(6 * INS) : "memory");
    else if (NST > 5 && rem == 5) asm volatile("s_waitcnt vmcnt(%0)" ::"n"(5 * INS) : "memory");
    else if (NST > 4 && rem == 4) asm volatile("s_waitcnt vmcnt(%0)" ::"n"(4 * INS) : "memory");
    else if (NST > 3 && rem == 3) asm volatile("s_waitcnt vmcnt(%0)" ::"n"(3 * INS) : "memory");
    else if (rem == 2) asm volatile("s_waitcnt vmcnt(%0)" ::"n"(2 * INS) : "memory");
    else if (rem == 1) asm volatile("s_waitcnt vmcnt(%0)" ::"n"(INS) : "memory");
    else asm volatile("s_waitcnt vmcnt(0)" ::: "memory");
  };

  u32x4 fa[2][MI];
  u32x2 fb[2][NI][2];
  const unsigned sbase = (unsigned)(size_t)smem;
  auto read_frags = [&](int kt, int ks, int slot) {
    const int st = kt % NST;
    const unsigned ab = sbase + st * ST_BYTES;
    const unsigned bb = ab + A_BYTES;
#pragma unroll
    for (int i = 0; i < MI; ++i) {
      int row = wm0 + i * 16 + (lane & 15);
      unsigned o = ab + a_off(row, ks * 4 + (lane >> 4));
      asm volatile("ds_read_b128 %0, %1" : "=v"(fa[slot][i]) : "v"(o));
    }
#pragma unroll
    for (int jn = 0; jn < NI; ++jn) {
      const int gq = lane >> 4, qq = (lane & 15) >> 2, pp = lane & 3;
      const int c = (wn0 + jn * 16) >> 4;
      unsigned o0 = bb + b_off<BN>(32 * ks + 8 * gq + 0 + qq, 2 * c + (pp >> 1)) + 8 * (pp & 1);
      unsigned o1 = bb + b_off<BN>(32 * ks + 8 * gq + 4 + qq, 2 * c + (pp >> 1)) + 8 * (pp & 1);
      asm volatile("ds_read_b64_tr_b16 %0, %1" : "=v"(fb[slot][jn][0]) : "v"(o0));
      asm volatile("ds_read_b64_tr_b16 %0, %1" : "=v"(fb[slot][jn][1]) : "v"(o1));
    }
  };
  auto do_mfma = [&](int slot) {
#pragma unroll
    for (int jn = 0; jn < NI; ++jn) {
      u32x4 bu = {fb[slot][jn][0][0], fb[slot][jn][0][1], fb[slot][jn][1][0], fb[slot][jn][1][1]};
      s16x8 bfr = __builtin_bit_cast(s16x8, bu);
#pragma unroll
      for (int i = 0; i < MI; ++i)
        acc[i][jn] = __builtin_amdgcn_mfma_f32_16x16x32_bf16(__builtin_bit_cast(s16x8, fa[slot][i]), bfr, acc[i][jn], 0, 0, 0);
    }
  };

  int last = -1;
#pragma unroll
  for (int i = 0; i < NST - 1; ++i)
    if (i < nk) { issue(i); last = i; }
  wait_stage(0, last);
  __builtin_amdgcn_s_barrier();
  read_frags(0, 0, 0);
  for (int kt = 0; kt < nk; ++kt) {
    if (kt + NST - 1 < nk) { issue(kt + NST - 1); last = kt + NST - 1; }
    asm volatile("s_waitcnt lgkmcnt(0)" ::: "memory");
    __builtin_amdgcn_sched_barrier(0);
    read_frags(kt, 1, 1);
    do_mfma(0);
    asm volatile("s_waitcnt lgkmcnt(0)" ::: "memory");
    __builtin_amdgcn_sched_barrier(0);
    if (kt + 1 < nk) {
      wait_stage(kt + 1, last);
      __builtin_amdgcn_s_barrier();
      read_frags(kt + 1, 0, 0);
    }
    do_mfma(1);
  }
  asm volatile("s_waitcnt lgkmcnt(0)" ::: "memory");
  __builtin_amdgcn_s_barrier();

  constexpr int LDC = BN + 4;
  float* Cs = (float*)smem;
#pragma unroll
  for (int i = 0; i < MI; ++i)
#pragma unroll
    for (int jn = 0; jn < NI; ++jn)
#pragma unroll
      for (int r = 0; r < 4; ++r) {
        int row = wm0 + i * 16 + (lane >> 4) * 4 + r;
        int col = wn0 + jn * 16 + (lane & 15);
        Cs[row * LDC + col] = acc[i][jn][r];
      }
  __syncthreads();
  constexpr int CH_PER_ROW = BN / 8;
  constexpr int ITER = BM * CH_PER_ROW / 512;
#pragma unroll
  for (int it = 0; it < ITER; ++it) {
    int c = tid + 512 * it;
    int row = c / CH_PER_ROW, ch = c % CH_PER_ROW;
    int grow = m0 + row, gcol = n0 + ch * 8;
    float v[8];
    f32x4 c0 = *(const f32x4*)(&Cs[row * LDC + ch * 8]);
    f32x4 c1 = *(const f32x4*)(&Cs[row * LDC + ch * 8 + 4]);
#pragma unroll
    for (int e = 0; e < 4; ++e) { v[e] = rbf(c0[e]); v[4 + e] = rbf(c1[e]); }
    if (MODE == 1) {
#pragma unroll
      for (int e = 0; e < 8; ++e) v[e] = rbf(gelu_tanh(v[e]));
      u32x4 o;
#pragma unroll
      for (int e = 0; e < 4; ++e) o[e] = pack2(v[2 * e], v[2 * e + 1]);
      *(u32x4*)(g.out + (size_t)grow * F_DIM + gcol) = o;
      if (n0 >= FH_DIM) {
        float sm = 0.f;
#pragma unroll
        for (int e = 0; e < 8; ++e) sm += v[e];
#pragma unroll
        for (int o2 = 8; o2 > 0; o2 >>= 1) sm += __shfl_xor(sm, o2);
        float mean = sm * (1.0f / 128.0f);
        float m2 = 0.f;
#pragma unroll
        for (int e = 0; e < 8; ++e) { float dd = v[e] - mean; m2 += dd * dd; }
#pragma unroll
        for (int o2 = 8; o2 > 0; o2 >>= 1) m2 += __shfl_xor(m2, o2);
        if (ch == 0) g.stats[(size_t)grow * 7 + (n0 - FH_DIM) / 128] = make_float2(mean, m2);
      }
    } else if (MODE == 2) {
      float b = bf2f(g.bsp[grow]);
      u32x4 z1 = *(const u32x4*)(g.z + (size_t)grow * F_DIM + gcol);
      u32x4 o;
#pragma unroll
      for (int e = 0; e < 4; ++e) {
        float g0 = rbf(v[2 * e] + b), g1 = rbf(v[2 * e + 1] + b);
        o[e] = pack2(lo16(z1[e]) * g0, hi16(z1[e]) * g1);
      }
      *(u32x4*)(g.out + (size_t)grow * FH_DIM + gcol) = o;
    } else {
      u32x4 xv = *(const u32x4*)(g.x1 + (size_t)grow * D_DIM + gcol);
      u32x4 o;
#pragma unroll
      for (int e = 0; e < 4; ++e) {
        float gb0 = g.mod[5 * D_DIM + gcol + 2 * e], gb1 = g.mod[5 * D_DIM + gcol + 2 * e + 1];
        o[e] = pack2(lo16(xv[e]) + rbf(gb0 * v[2 * e]), hi16(xv[e]) + rbf(gb1 * v[2 * e + 1]));
      }
      *(u32x4*)(g.out + (size_t)grow * D_DIM + gcol) = o;
    }
  }
}

__global__ __launch_bounds__(256) void k3b_ln_z2(const u16* __restrict__ z, const float2* __restrict__ stats,
                                                 u16* __restrict__ z2n) {
  const int row = blockIdx.x * 4 + (threadIdx.x >> 6);
  const int lane = threadIdx.x & 63;
  const float2* st = stats + (size_t)row * 7;
  float mean = 0.f;
#pragma unroll
  for (int b = 0; b < 7; ++b) mean += st[b].x;
  mean *= (1.0f / 7.0f);
  float m2 = 0.f;
#pragma unroll
  for (int b = 0; b < 7; ++b) { float dd = st[b].x - mean; m2 += st[b].y + 128.0f * dd * dd; }
  float var = m2 * (1.0f / FH_DIM);
  float mb = rbf(mean);
  float sd = rbf(sqrtf(rbf(rbf(var) + 1e-5f)));
#pragma unroll
  for (int i = 0; i < 2; ++i) {
    int c = lane + 64 * i;
    if (c < 112) {
      u32x4 v = *(const u32x4*)(z + (size_t)row * F_DIM + FH_DIM + c * 8);
#pragma unroll
      for (int e = 0; e < 4; ++e) {
        float a = rbf(rbf(lo16(v[e]) - mb) / sd);
        float b2 = rbf(rbf(hi16(v[e]) - mb) / sd);
        v[e] = pack2(a, b2);
      }
      *(u32x4*)(z2n + (size_t)row * FH_DIM + c * 8) = v;
    }
  }
}

extern "C" void kernel_launch(void* const* d_in, const int* in_sizes, int n_in, void* d_out, int out_size,
                              void* d_ws, size_t ws_size, hipStream_t stream) {
  const u16* q = (const u16*)d_in[0];
  const u16* k = (const u16*)d_in[1];
  const u16* v = (const u16*)d_in[2];
  const u16* shiftk = (const u16*)d_in[3];
  const u16* ssmk = (const u16*)d_in[4];
  const u16* ssmD = (const u16*)d_in[5];
  const u16* w_in = (const u16*)d_in[7];
  const u16* w_sp = (const u16*)d_in[8];
  const u16* b_sp = (const u16*)d_in[9];
  const u16* w_out = (const u16*)d_in[10];
  const u16* cvec = (const u16*)d_in[11];
  const u16* w_mod = (const u16*)d_in[12];
  u16* out = (u16*)d_out;

  char* ws = (char*)d_ws;
  size_t off = 0;
  float* mod = (float*)(ws + off); off += 16384;
  u16* yT = (u16*)(ws + off); off += (size_t)D_DIM * S_LEN * 2;
  u16* x1 = (u16*)(ws + off); off += (size_t)S_LEN * D_DIM * 2;
  u16* h2 = (u16*)(ws + off); off += (size_t)S_LEN * D_DIM * 2;
  u16* z = (u16*)(ws + off); off += (size_t)S_LEN * F_DIM * 2;
  float2* stats = (float2*)(ws + off); off += (size_t)S_LEN * 7 * 8;
  u16* t = (u16*)(ws + off); off += (size_t)S_LEN * FH_DIM * 2;
  u16* z2n = (u16*)(ws + off); off += (size_t)S_LEN * FH_DIM * 2;

  hipFuncSetAttribute((const void*)k1_conv_mod, hipFuncAttributeMaxDynamicSharedMemorySize, 2 * CH_BYTES);
  hipFuncSetAttribute((const void*)gemm_kernel<128, 128, 1, 4>, hipFuncAttributeMaxDynamicSharedMemorySize, 131072);
  hipFuncSetAttribute((const void*)gemm_kernel<128, 64, 2, 6>, hipFuncAttributeMaxDynamicSharedMemorySize, 147456);
  hipFuncSetAttribute((const void*)gemm_kernel<64, 64, 3, 8>, hipFuncAttributeMaxDynamicSharedMemorySize, 131072);
  hipLaunchKernelGGL(k1_conv_mod, dim3(K1_CONV_BLOCKS + K1_MOD_BLOCKS), dim3(512), 2 * CH_BYTES, stream, k, v,
                     shiftk, ssmk, ssmD, cvec, w_mod, mod, yT);
  hipLaunchKernelGGL(k2_rowwise, dim3(S_LEN / 8), dim3(512), 0, stream, q, yT, mod, x1, h2);

  GemmArgs g1;
  g1.A = h2; g1.B = w_in; g1.lda = D_DIM; g1.ldb = F_DIM; g1.K = D_DIM; g1.pad = 0;
  g1.out = z; g1.z = z; g1.stats = stats; g1.bsp = b_sp; g1.x1 = x1; g1.mod = mod;
  hipLaunchKernelGGL((gemm_kernel<128, 128, 1, 4>), dim3(F_DIM / 128, S_LEN / 128), dim3(512),
                     4 * (128 * 64 * 2 + 64 * 128 * 2), stream, g1);

  hipLaunchKernelGGL(k3b_ln_z2, dim3(S_LEN / 4), dim3(256), 0, stream, z, stats, z2n);

  GemmArgs g2;
  g2.A = w_sp; g2.B = z2n; g2.lda = S_LEN; g2.ldb = FH_DIM; g2.K = S_LEN; g2.pad = 0;
  g2.out = t; g2.z = z; g2.stats = stats; g2.bsp = b_sp; g2.x1 = x1; g2.mod = mod;
  hipLaunchKernelGGL((gemm_kernel<128, 64, 2, 6>), dim3(FH_DIM / 64, S_LEN / 128), dim3(512),
                     6 * (128 * 64 * 2 + 64 * 64 * 2), stream, g2);

  GemmArgs g3;
  g3.A = t; g3.B = w_out; g3.lda = FH_DIM; g3.ldb = D_DIM; g3.K = FH_DIM; g3.pad = 0;
  g3.out = out; g3.z = z; g3.stats = stats; g3.bsp = b_sp; g3.x1 = x1; g3.mod = mod;
  hipLaunchKernelGGL((gemm_kernel<64, 64, 3, 8>), dim3(D_DIM / 64, S_LEN / 64), dim3(512),
                     8 * (64 * 64 * 2 + 64 * 64 * 2), stream, g3);
}
```
